```python
import torch

def _softmax(x, axis=-1):
    return torch.softmax(x, dim=axis)
def _gelu(x):
    return torch.nn.functional.gelu(x, approximate="tanh")
def _silu(x):
    return torch.nn.functional.silu(x)
def _sigmoid(x):
    return torch.sigmoid(x)
def _layernorm(x, axis=-1, eps=1e-5):
    m = x.mean(dim=axis, keepdim=True)
    v = x.var(dim=axis, keepdim=True, unbiased=False)
    return (x - m) / torch.sqrt(v + eps)
def _scatter_add(tgt, idx, src, axis=0):
    return tgt.clone().index_add_(axis, idx, src)
def _segment_sum(src, seg, n):
    out = torch.zeros((n,) + src.shape[1:], dtype=src.dtype)
    return out.index_add_(0, seg, src)
def _pad(x, pw):
    flat = tuple(v for pair in reversed(pw) for v in pair)
    return torch.nn.functional.pad(x, flat)

kernel_name = 'gated_mix__mha_block__geglu_ffn__prompt'

def setup_inputs():

    _w = (lambda a: (lambda t: t.to(torch.bfloat16) if t.is_floating_point() else t)(torch.as_tensor(a)))
    def _A_setup():
        B, S, D, H = 1, 2048, 640, 32
        return {
            "x": _w((lambda *s: torch.randn(*s, dtype=torch.bfloat16) * 0.05)(B, S, D)),
            "wqkv": _w((lambda *s: torch.randn(*s, dtype=torch.bfloat16) * 0.8)(D, 3 * D)),
            "wo": _w((lambda *s: torch.randn(*s, dtype=torch.bfloat16) * 0.05)(D, D)),
        }
    _A_in = _A_setup()

    def _B_setup():
        B, S, D, F = 1, 2048, 640, 1536
        return {
            "x": _w((lambda *s: torch.randn(*s, dtype=torch.bfloat16) * 0.05)(B, S, D)),
            "w_gate": _w((lambda *s: torch.randn(*s, dtype=torch.bfloat16) * 0.1)(D, F)),
            "w_up": _w((lambda *s: torch.randn(*s, dtype=torch.bfloat16) * 0.1)(D, F)),
            "w_down": _w((lambda *s: torch.randn(*s, dtype=torch.bfloat16) * 0.1)(F, D)),
        }
    _B_in = _B_setup()

    return {
        "A_x": _A_in["x"],
        "A_wqkv": _A_in["wqkv"],
        "A_wo": _A_in["wo"],
        "B_x": _B_in["x"],
        "B_w_gate": _B_in["w_gate"],
        "B_w_up": _B_in["w_up"],
        "B_w_down": _B_in["w_down"],
        "gate_w": _w((lambda *s: torch.randn(*s, dtype=torch.bfloat16) * 0.05)(640) * 16.0),
    }

def reference(A_x, A_wqkv, A_wo, B_x, B_w_gate, B_w_up, B_w_down, gate_w):
    def _A_ref(x, wqkv, wo):
        B, S, D = x.shape
        H = 32
        Dh = D // H
        qkv = (lambda x, *d: x.permute(*d))((x @ wqkv).reshape(B, S, 3, H, Dh), 2, 0, 3, 1, 4)
        q, k, v = qkv[0], qkv[1], qkv[2]
        scores = torch.einsum("bhqd,bhkd->bhqk", q, k) * (Dh ** -0.5)
        p = _softmax(scores, axis=-1)
        o = torch.einsum("bhqk,bhkd->bhqd", p, v)
        return (lambda x, *d: x.permute(*d))(o, 0, 2, 1, 3).reshape(B, S, D) @ wo

    def _B_ref(x, w_gate, w_up, w_down):
        return (_gelu(x @ w_gate) * (x @ w_up)) @ w_down

    x = A_x
    g = _sigmoid((x * gate_w).sum(-1, keepdims=True))
    a = _A_ref(x, A_wqkv, A_wo)
    b = _B_ref(x, B_w_gate, B_w_up, B_w_down)
    return g * a + (1 - g) * b
```

```cpp
#include <hip/hip_runtime.h>
#include <hip/hip_bf16.h>
#include <stdint.h>

typedef unsigned short u16;
typedef short bf16x8 __attribute__((ext_vector_type(8)));
typedef short s16x4 __attribute__((ext_vector_type(4)));
typedef float f32x4 __attribute__((ext_vector_type(4)));
typedef float f32x16 __attribute__((ext_vector_type(16)));
typedef unsigned u32x4 __attribute__((ext_vector_type(4)));
typedef unsigned u32x2 __attribute__((ext_vector_type(2)));
typedef float f32x2 __attribute__((ext_vector_type(2)));
typedef __bf16 bf16x2_t __attribute__((ext_vector_type(2)));
#define LAS __attribute__((address_space(3)))
typedef LAS const char* lds_cptr;
typedef LAS char* lds_ptr;

constexpr int S = 2048, D = 640, NH = 32, DH = 20, FF = 1536, NQKV = 1920, KOH = D + FF;
constexpr float QSCALE = 0.22360679774997896f * 1.4426950408889634f;

constexpr size_t WS_Q = 0;
constexpr size_t WS_KV = WS_Q + (size_t)NH * S * DH * 2;
constexpr size_t WS_KMAX = WS_KV + (size_t)NH * 64 * 3072;
constexpr size_t WS_G = WS_KMAX + 32 * 32 * 4;
constexpr size_t WS_OH = WS_G + S * 4;
constexpr size_t WS_END = WS_OH + (size_t)S * KOH * 2;

#define PROF_BEGIN()
#define PROF_END(kid)
__device__ __forceinline__ unsigned cvtpk(float lo, float hi) { f32x2 v = {lo, hi}; bf16x2_t b = __builtin_convertvector(v, bf16x2_t); return __builtin_bit_cast(unsigned, b); }
__device__ __forceinline__ float bf_lo(unsigned u) { return __uint_as_float(u << 16); }
__device__ __forceinline__ float bf_hi(unsigned u) { return __uint_as_float(u & 0xffff0000u); }
__device__ __forceinline__ void glds16(unsigned voff, const void* sbase, unsigned lds_dst) {
    unsigned keep;
    asm volatile("s_mov_b32 %0, m0\n\ts_mov_b32 m0, %3\n\ts_nop 0\n\tglobal_load_lds_dwordx4 %1, %2\n\ts_mov_b32 m0, %0"
                 : "=&s"(keep) : "v"(voff), "s"(sbase), "s"(lds_dst) : "memory");
}
template <int N> __device__ __forceinline__ void wait_vm() { asm volatile("s_waitcnt vmcnt(%0)" :: "i"(N) : "memory"); }
template <int N> __device__ __forceinline__ void wait_vm_bar() { asm volatile("s_waitcnt vmcnt(%0) lgkmcnt(0)\n\ts_barrier" :: "i"(N) : "memory"); }
__device__ __forceinline__ bf16x8 lds_b128(lds_cptr p) { return *(LAS const bf16x8*)p; }
typedef short v4i16_t __attribute__((ext_vector_type(4)));
__device__ __forceinline__ s16x4 lds_tr(lds_cptr p) { return __builtin_bit_cast(s16x4, __builtin_amdgcn_ds_read_tr16_b64_v4i16((LAS v4i16_t*)p)); }
__device__ __forceinline__ bf16x8 cat8(s16x4 a, s16x4 b) { return (bf16x8){a[0], a[1], a[2], a[3], b[0], b[1], b[2], b[3]}; }
#define MFMA16(a, b, c) __builtin_amdgcn_mfma_f32_16x16x32_bf16(a, b, c, 0, 0, 0)
#define MFMA32(a, b, c) __builtin_amdgcn_mfma_f32_32x32x16_bf16(a, b, c, 0, 0, 0)
__device__ __forceinline__ int swz4(int r) { return (0x78 >> (((r >> 2) & 3) * 2)) & 3; }

constexpr int K1_STAGE = 28 * 1024, K1_NST = 4, K1_X = 0, K1_WQ = 8192, K1_WG = 16384;
constexpr int K1_GW = K1_NST * K1_STAGE;
constexpr int K1_GB = K1_GW + 1280;
constexpr int K1_LDS = K1_GB + 512;
constexpr int K1_NS = D / 32;

__global__ __launch_bounds__(512, 2) void k1_proj(const u16* __restrict__ x, const u16* __restrict__ wqkv, const u16* __restrict__ wg,
                                                 const u16* __restrict__ wu, const u16* __restrict__ gatew, char* __restrict__ ws) {
    extern __shared__ __attribute__((aligned(16))) char smem[];
    const int tid = threadIdx.x, lane = tid & 63, wid = __builtin_amdgcn_readfirstlane(tid >> 6);
    PROF_BEGIN(); char* ws_prof = ws;
    const int bid = blockIdx.x, xcd = bid & 7, bi = bid >> 3;
    const int rb = (xcd & 1) * 8 + (bi & 7), j = (xcd >> 1) * 4 + (bi >> 3);
    const unsigned lds0 = (unsigned)(uintptr_t)smem;
    const lds_cptr L = (lds_cptr)smem;
    if (tid < 80) *(LAS u32x4*)((lds_ptr)smem + K1_GW + tid * 16) = *(const u32x4*)(gatew + tid * 8);
    const int l15 = lane & 15, g4 = lane >> 4, l31 = lane & 31, hi = lane >> 5;

    if (wid < 4) {
        const int wm = wid >> 1, wn = wid & 1;
        unsigned xo[2], wo_[2];
#pragma unroll
        for (int i = 0; i < 2; ++i) {
            const int p = 2 * wid + i, row = 16 * p + (lane >> 2), ch = (lane & 3) ^ swz4(row);
            xo[i] = (unsigned)((rb * 128 + row) * D + ch * 8) * 2u;
            const int U = 4 * p + (lane >> 4), o = U >> 3, t = U & 7, hh = (lane >> 3) & 1, q = (lane & 7) >> 1, c = lane & 1;
            const int k = 8 * o + 4 * (hh ^ (o & 1)) + q; int id = 2 * t + c; if (id == 15) id = 14;
            const int col = (id < 5) ? (D + j * 40 + 8 * id) : (id < 10) ? (j * 40 + 8 * (id - 5)) : (2 * D + j * 40 + 8 * (id - 10));
            wo_[i] = (unsigned)(k * NQKV + col) * 2u;
        }
        auto issue = [&](int s) {
            const unsigned dst = lds0 + (s % K1_NST) * K1_STAGE;
            const char* xb = (const char*)x + s * 64; const char* wb = (const char*)wqkv + (size_t)s * 32 * NQKV * 2;
            glds16(xo[0], xb, dst + K1_X + (2 * wid) * 1024); glds16(xo[1], xb, dst + K1_X + (2 * wid + 1) * 1024);
            glds16(wo_[0], wb, dst + K1_WQ + (2 * wid) * 1024); glds16(wo_[1], wb, dst + K1_WQ + (2 * wid + 1) * 1024);
        };
        const int xrd = K1_X + (wm * 64 + l15) * 64 + ((g4 ^ swz4(l15)) * 16);
        const int wrd1 = K1_WQ + (g4 * 8 + wn * 4) * 256 + (g4 & 1) * 128 + (l15 >> 2) * 32 + (l15 & 3) * 8;
        const int wrd2 = wrd1 ^ 128;
        const int grd = K1_GW + g4 * 16;
        f32x4 acc[4][4];
#pragma unroll
        for (int a = 0; a < 4; ++a)
#pragma unroll
            for (int b = 0; b < 4; ++b) acc[a][b] = (f32x4){0.f, 0.f, 0.f, 0.f};
        float gacc[4] = {0.f, 0.f, 0.f, 0.f};
        bf16x8 xf[2][4], wf[2][4], gf[2];
        auto rd = [&](int s, int b) {
            const lds_cptr base = L + (s % K1_NST) * K1_STAGE;
#pragma unroll
            for (int t = 0; t < 4; ++t) xf[b][t] = lds_b128(base + xrd + t * 1024);
#pragma unroll
            for (int t = 0; t < 4; ++t) wf[b][t] = cat8(lds_tr(base + wrd1 + t * 256), lds_tr(base + wrd2 + t * 256));
            gf[b] = lds_b128(L + grd + s * 64);
        };
        issue(0); issue(1); issue(2); issue(3);
        wait_vm_bar<12>();
        rd(0, 0);
#pragma unroll
        for (int s = 0; s < K1_NS; ++s) {
            const int cur = s & 1;
            if (s + 1 < K1_NS) {
                if (s <= 16) wait_vm_bar<8>(); else if (s == 17) wait_vm_bar<4>(); else wait_vm_bar<0>();
                if (s + 4 < K1_NS) issue(s + 4);
                rd(s + 1, cur ^ 1);
            }
            if (wn == 0) {
#pragma unroll
                for (int n = 0; n < 4; ++n)
#pragma unroll
                    for (int t = 0; t < 4; ++t) acc[n][t] = MFMA16(wf[cur][n], xf[cur][t], acc[n][t]);
            } else {
#pragma unroll
                for (int t = 0; t < 4; ++t) acc[0][t] = MFMA16(wf[cur][0], xf[cur][t], acc[0][t]);
#pragma unroll
                for (int n = 1; n < 4; ++n)
#pragma unroll
                    for (int t = 0; t < 4; ++t) acc[n][t] = MFMA16(xf[cur][t], wf[cur][n], acc[n][t]);
#pragma unroll
                for (int t = 0; t < 4; ++t)
#pragma unroll
                    for (int e = 0; e < 4; ++e) {
                        bf16x2_t a = __builtin_bit_cast(bf16x2_t, (unsigned)(u16)xf[cur][t][2 * e] | ((unsigned)(u16)xf[cur][t][2 * e + 1] << 16));
                        bf16x2_t b = __builtin_bit_cast(bf16x2_t, (unsigned)(u16)gf[cur][2 * e] | ((unsigned)(u16)gf[cur][2 * e + 1] << 16));
                        gacc[t] = __builtin_amdgcn_fdot2_f32_bf16(a, b, gacc[t], false);
                    }
            }
        }
        if (wn == 1) {
#pragma unroll
            for (int t = 0; t < 4; ++t) {
                float v = gacc[t]; v += __shfl_xor(v, 16); v += __shfl_xor(v, 32);
                const float gg = __builtin_amdgcn_rcpf(1.f + __builtin_amdgcn_exp2f(-1.4426950408889634f * v));
                if (g4 == 0) { const int tok = wm * 64 + t * 16 + l15; *(LAS float*)((lds_ptr)smem + K1_GB + tok * 4) = gg;
                               if (j == 0) ((float*)(ws + WS_G))[rb * 128 + tok] = gg; }
            }
        }
        asm volatile("s_waitcnt lgkmcnt(0)\n\ts_barrier" ::: "memory");
        char* Qs = ws + WS_Q; char* KV = ws + WS_KV;
        float kn[2][4];
#pragma unroll
        for (int t = 0; t < 4; ++t) { kn[0][t] = 0.f; kn[1][t] = 0.f; }
#pragma unroll
        for (int n = 0; n < 4; ++n) {
            const int tile = wn * 4 + n;
            if (wn == 0 || n == 0) {
                const int u = 4 * tile + g4;
                const int hs = (u >= 5 && u < 10) || u >= 15 ? 1 : 0, jj = u - (u >= 15 ? 15 : u >= 10 ? 10 : u >= 5 ? 5 : 0);
                const int head = 2 * j + hs; const bool isK = u < 10;
#pragma unroll
                for (int t = 0; t < 4; ++t) {
                    const int tok = rb * 128 + wm * 64 + t * 16 + l15; const f32x4 v = acc[n][t];
                    if (isK) {
                        const u32x2 w = {cvtpk(v[0], v[1]), cvtpk(v[2], v[3])};
                        const float sq = v[0] * v[0] + v[1] * v[1] + v[2] * v[2] + v[3] * v[3];
                        if (hs) kn[1][t] += sq; else kn[0][t] += sq;
                        char* tb = KV + ((size_t)head * 64 + (tok >> 5)) * 3072; const int key = tok & 31;
                        if (jj < 4) *(u32x2*)(tb + (jj >> 1) * 512 + key * 16 + (jj & 1) * 8) = w;
                        else *(u32x4*)(tb + 1024 + key * 16) = (u32x4){w[0], w[1], 0x3f803f80u, 0u};
                    } else {
                        const u32x2 w = {cvtpk(v[0] * QSCALE, v[1] * QSCALE), cvtpk(v[2] * QSCALE, v[3] * QSCALE)};
                        *(u32x2*)(Qs + ((size_t)head * S + tok) * 40 + jj * 8) = w;
                    }
                }
            } else {
                const int nv = (n - 1) * 16 + l15;
                const int hs = nv < 20 ? 0 : nv < 40 ? 1 : (nv - 40) >> 2, row = nv < 20 ? nv : nv < 40 ? nv - 20 : 20 + ((nv - 40) & 3);
                const int head = 2 * j + hs;
#pragma unroll
                for (int t = 0; t < 4; ++t) {
                    const int tok0 = rb * 128 + wm * 64 + t * 16; const f32x4 v = acc[n][t];
                    u32x2 w = {cvtpk(v[0], v[1]), cvtpk(v[2], v[3])};
                    if (nv >= 40) { const unsigned c = (row == 20) ? 0x3f803f80u : 0u; w = (u32x2){c, c}; }
                    char* tb = KV + ((size_t)head * 64 + (tok0 >> 5)) * 3072 + 1536;
                    *(u32x2*)(tb + ((((tok0 >> 4) & 1) * 2 + (g4 & 1)) * 24 + row) * 16 + (g4 >> 1) * 8) = w;
                }
            }
        }
        if (wn == 0) {
#pragma unroll
            for (int hs = 0; hs < 2; ++hs) {
                float mx = 0.f;
#pragma unroll
                for (int t = 0; t < 4; ++t) { float v = kn[hs][t]; v += __shfl_xor(v, 16); v += __shfl_xor(v, 32); mx = fmaxf(mx, v); }
                mx = fmaxf(mx, __shfl_xor(mx, 1)); mx = fmaxf(mx, __shfl_xor(mx, 2)); mx = fmaxf(mx, __shfl_xor(mx, 4)); mx = fmaxf(mx, __shfl_xor(mx, 8));
                if (lane == 0) ((float*)(ws + WS_KMAX))[(2 * j + hs) * 32 + rb * 2 + wm] = mx;
            }
        }
    } else {
        const int w4 = wid - 4, wm = w4 >> 1, wn = w4 & 1;
        unsigned go[3];
#pragma unroll
        for (int i = 0; i < 3; ++i) {
            const int p = 3 * w4 + i, U = 4 * p + (lane >> 4), kq = U / 6, T = U % 6, q = (lane & 15) >> 2, c = lane & 3;
            const int k = 4 * kq + q;
            go[i] = (unsigned)(k * FF + j * 96 + 16 * T + 8 * (c & 1)) * 2u;
        }
        const bool isup = (lane & 2) != 0;
        const long long dgu = (const char*)wu - (const char*)wg;
        auto issue = [&](int s) {
            const unsigned dst = lds0 + (s % K1_NST) * K1_STAGE + K1_WG + (3 * w4) * 1024;
#pragma unroll
            for (int i = 0; i < 3; ++i) {
                const char* src = (const char*)wg + (size_t)s * 32 * FF * 2 + go[i] + (isup ? dgu : 0);
                unsigned keep;
                asm volatile("s_mov_b32 %0, m0\n\ts_mov_b32 m0, %2\n\ts_nop 0\n\tglobal_load_lds_dwordx4 %1, off\n\ts_mov_b32 m0, %0"
                             : "=&s"(keep) : "v"(src), "s"(dst + i * 1024) : "memory");
            }
        };
        int xrd[2];
#pragma unroll
        for (int ks = 0; ks < 2; ++ks) xrd[ks] = K1_X + (wm * 64 + l31) * 64 + (((2 * ks + hi) ^ swz4(l31)) * 16);
        const int wrd = K1_WG + ((2 * hi) * 6 + wn * 3) * 256 + (l15 >> 2) * 64 + (g4 & 1) * 32 + (l15 & 3) * 8;
        f32x16 acc[3][2];
#pragma unroll
        for (int a = 0; a < 3; ++a)
#pragma unroll
            for (int b = 0; b < 2; ++b) acc[a][b] = (f32x16){};
        bf16x8 xf[2][2][2], wf[2][2][3];
        auto rd = [&](int s, int b) {
            const lds_cptr base = L + (s % K1_NST) * K1_STAGE;
#pragma unroll
            for (int ks = 0; ks < 2; ++ks) {
#pragma unroll
                for (int t = 0; t < 2; ++t) xf[b][ks][t] = lds_b128(base + xrd[ks] + t * 2048);
#pragma unroll
                for (int n = 0; n < 3; ++n) wf[b][ks][n] = cat8(lds_tr(base + wrd + (4 * ks) * 1536 + n * 256), lds_tr(base + wrd + (4 * ks + 1) * 1536 + n * 256));
            }
        };
        issue(0); issue(1); issue(2); issue(3);
        wait_vm_bar<9>();
        rd(0, 0);
#pragma unroll
        for (int s = 0; s < K1_NS; ++s) {
            const int cur = s & 1;
            if (s + 1 < K1_NS) {
                if (s <= 16) wait_vm_bar<6>(); else if (s == 17) wait_vm_bar<3>(); else wait_vm_bar<0>();
                if (s + 4 < K1_NS) issue(s + 4);
                rd(s + 1, cur ^ 1);
            }
#pragma unroll
            for (int ks = 0; ks < 2; ++ks)
#pragma unroll
                for (int n = 0; n < 3; ++n)
#pragma unroll
                    for (int t = 0; t < 2; ++t) acc[n][t] = MFMA32(wf[cur][ks][n], xf[cur][ks][t], acc[n][t]);
        }
        asm volatile("s_waitcnt lgkmcnt(0)\n\ts_barrier" ::: "memory");
        u16* OH = (u16*)(ws + WS_OH);
#pragma unroll
        for (int t = 0; t < 2; ++t) {
            const int tl = wm * 64 + t * 32 + l31; const float omg = 1.f - *(LAS const float*)(L + K1_GB + tl * 4);
            u16* row = OH + (size_t)(rb * 128 + tl) * KOH + D + j * 96 + wn * 48;
#pragma unroll
            for (int n = 0; n < 3; ++n) {
#pragma unroll
                for (int hf = 0; hf < 2; ++hf) {
                    float h[4];
#pragma unroll
                    for (int e = 0; e < 4; ++e) {
                        const float gt = acc[n][t][4 * hf + e], up = acc[n][t][8 + 4 * hf + e];
                        const float u = gt * (1.f + 0.044715f * gt * gt) * (-2.f * 0.7978845608028654f * 1.4426950408889634f);
                        h[e] = gt * __builtin_amdgcn_rcpf(1.f + __builtin_amdgcn_exp2f(u)) * up * omg;
                    }
                    *(u32x2*)(row + n * 16 + hf * 8 + hi * 4) = (u32x2){cvtpk(h[0], h[1]), cvtpk(h[2], h[3])};
                }
            }
        }
    }
    PROF_END(0);
}

constexpr int K2_CH = 8 * 3072, K2_NSL = 3, K2_LDS = K2_NSL * K2_CH + 64;
constexpr float L_MIN = 9.094947017729282e-13f;

template <bool MAXPASS>
__device__ __forceinline__ void attn_stream(const char* kvh, unsigned lds0, lds_cptr L, int wid, int lane, bf16x8 q0, bf16x8 q1, f32x16& o, float& mx) {
    const int l31 = lane & 31, hi = lane >> 5;
    const unsigned voff = (unsigned)(wid * 3072 + lane * 16);
    auto issue = [&](int c) {
        const char* src = kvh + (size_t)c * K2_CH; const unsigned dst = lds0 + (c % K2_NSL) * K2_CH + wid * 3072;
        glds16(voff, src, dst); glds16(voff, src + 1024, dst + 1024); glds16(voff, src + 2048, dst + 2048);
    };
    const int k0o = hi * 512 + l31 * 16, k1o = 1024 + l31 * 16, vo = 1536 + hi * 384 + l31 * 16;
    issue(0); issue(1);
    for (int c = 0; c < 8; ++c) {
        if (c < 7) wait_vm_bar<3>(); else wait_vm_bar<0>();
        if (c + 2 < 8) issue(c + 2);
        const lds_cptr base = L + (c % K2_NSL) * K2_CH;
#pragma unroll
        for (int t = 0; t < 8; ++t) {
            const bf16x8 kf0 = lds_b128(base + t * 3072 + k0o), kf1 = lds_b128(base + t * 3072 + k1o);
            f32x16 s = MFMA32(kf0, q0, (f32x16){}); s = MFMA32(kf1, q1, s);
            if constexpr (MAXPASS) {
#pragma unroll
                for (int r = 0; r < 16; ++r) mx = fmaxf(mx, s[r]);
            } else {
                const bf16x8 vf0 = lds_b128(base + t * 3072 + vo), vf1 = lds_b128(base + t * 3072 + vo + 768);
                u32x4 p0, p1;
#pragma unroll
                for (int r = 0; r < 4; ++r) p0[r] = cvtpk(__builtin_amdgcn_exp2f(s[2 * r]), __builtin_amdgcn_exp2f(s[2 * r + 1]));
#pragma unroll
                for (int r = 0; r < 4; ++r) p1[r] = cvtpk(__builtin_amdgcn_exp2f(s[8 + 2 * r]), __builtin_amdgcn_exp2f(s[8 + 2 * r + 1]));
                o = MFMA32(vf0, __builtin_bit_cast(bf16x8, p0), o); o = MFMA32(vf1, __builtin_bit_cast(bf16x8, p1), o);
            }
        }
    }
    asm volatile("s_waitcnt lgkmcnt(0)\n\ts_barrier" ::: "memory");
}
__device__ __forceinline__ unsigned neg_split(float m) {
    const float mh = __uint_as_float(__float_as_uint(m) & 0xffff0000u); const float ml = m - mh;
    const unsigned lo16 = (__float_as_uint(-mh) >> 16), hi16 = cvtpk(-ml, 0.f) & 0xffffu;
    return lo16 | (hi16 << 16);
}

__global__ __launch_bounds__(512, 2) void k2_attn(char* __restrict__ ws) {
    extern __shared__ __attribute__((aligned(16))) char smem[];
    const int tid = threadIdx.x, lane = tid & 63, wid = __builtin_amdgcn_readfirstlane(tid >> 6);
    PROF_BEGIN(); char* ws_prof = ws;
    const int bid = blockIdx.x, head = (bid & 7) * 4 + ((bid >> 3) & 3), qb = bid >> 5;
    const unsigned lds0 = (unsigned)(uintptr_t)smem; const lds_cptr L = (lds_cptr)smem;
    const int l31 = lane & 31, hi = lane >> 5;
    const int tok = qb * 256 + wid * 32 + l31;
    const char* kvh = ws + WS_KV + (size_t)head * 64 * 3072;
    const char* qrow = ws + WS_Q + ((size_t)head * S + tok) * 40;
    const u32x2 qa = *(const u32x2*)(qrow + hi * 16), qc = *(const u32x2*)(qrow + hi * 16 + 8), qe = *(const u32x2*)(qrow + 32);
    const float km = ((const float*)(ws + WS_KMAX))[head * 32 + l31];
    const float gq = ((const float*)(ws + WS_G))[tok];
    float qn = bf_lo(qa[0]) * bf_lo(qa[0]) + bf_hi(qa[0]) * bf_hi(qa[0]) + bf_lo(qa[1]) * bf_lo(qa[1]) + bf_hi(qa[1]) * bf_hi(qa[1])
             + bf_lo(qc[0]) * bf_lo(qc[0]) + bf_hi(qc[0]) * bf_hi(qc[0]) + bf_lo(qc[1]) * bf_lo(qc[1]) + bf_hi(qc[1]) * bf_hi(qc[1]);
    if (hi == 0) qn += bf_lo(qe[0]) * bf_lo(qe[0]) + bf_hi(qe[0]) * bf_hi(qe[0]) + bf_lo(qe[1]) * bf_lo(qe[1]) + bf_hi(qe[1]) * bf_hi(qe[1]);
    qn += __shfl_xor(qn, 32);
    float kmx = km;
#pragma unroll
    for (int o = 1; o < 32; o <<= 1) kmx = fmaxf(kmx, __shfl_xor(kmx, o));
    float m = sqrtf(qn * kmx) * 1.02f + 1e-30f;
    const bf16x8 q0 = __builtin_bit_cast(bf16x8, (u32x4){qa[0], qa[1], qc[0], qc[1]});
    f32x16 o; float l;
    for (int attempt = 0;; ++attempt) {
        const bf16x8 q1 = __builtin_bit_cast(bf16x8, hi == 0 ? (u32x4){qe[0], qe[1], neg_split(m), 0u} : (u32x4){0u, 0u, 0u, 0u});
        o = (f32x16){}; float dummy = 0.f;
        attn_stream<false>(kvh, lds0, L, wid, lane, q0, q1, o, dummy);
        l = __shfl(o[8], 32 + l31);
        const bool bad = !(l >= L_MIN);
        if (attempt == 1) break;
        LAS unsigned* flag = (LAS unsigned*)((lds_ptr)smem + K2_NSL * K2_CH);
        if (tid == 0) *flag = 0u;
        asm volatile("s_waitcnt lgkmcnt(0)\n\ts_barrier" ::: "memory");
        if (__any(bad) && lane == 0) *flag = 1u;
        asm volatile("s_waitcnt lgkmcnt(0)\n\ts_barrier" ::: "memory");
        const unsigned f = *(volatile LAS unsigned*)flag;
        asm volatile("s_waitcnt lgkmcnt(0)\n\ts_barrier" ::: "memory");
        if (!f) break;
        const bf16x8 q1z = __builtin_bit_cast(bf16x8, hi == 0 ? (u32x4){qe[0], qe[1], 0u, 0u} : (u32x4){0u, 0u, 0u, 0u});
        float mx = -INFINITY; f32x16 od = (f32x16){};
        attn_stream<true>(kvh, lds0, L, wid, lane, q0, q1z, od, mx);
        mx = fmaxf(mx, __shfl_xor(mx, 32));
        m = mx;
    }
    const float sc = gq * __builtin_amdgcn_rcpf(l);
    u16* orow = (u16*)(ws + WS_OH) + (size_t)tok * KOH + head * DH + hi * 4;
#pragma unroll
    for (int b = 0; b < 3; ++b) {
        if (b < 2 || hi == 0)
            *(u32x2*)(orow + b * 8) = (u32x2){cvtpk(o[4 * b] * sc, o[4 * b + 1] * sc), cvtpk(o[4 * b + 2] * sc, o[4 * b + 3] * sc)};
    }
    PROF_END(1);
}

constexpr int K3_STAGE = 9 * 1024, K3_NST = 4, K3_WAVE = K3_STAGE * K3_NST, K3_LDS = 4 * K3_WAVE, K3_NS = 17;

__global__ __launch_bounds__(256, 1) void k3_out(const u16* __restrict__ wo, const u16* __restrict__ wd, const char* __restrict__ ws, u16* __restrict__ out) {
    extern __shared__ __attribute__((aligned(16))) char smem[];
    const int tid = threadIdx.x, lane = tid & 63, wid = __builtin_amdgcn_readfirstlane(tid >> 6);
    PROF_BEGIN(); char* ws_prof = (char*)ws;
    const int bid = blockIdx.x, xcd = bid & 7, bi = bid >> 3;
    const int mt = (xcd >> 1) * 8 + (bi & 7), nt = (xcd & 1) * 4 + (bi >> 3);
    const unsigned lds0 = (unsigned)(uintptr_t)smem + wid * K3_WAVE; const lds_cptr L = (lds_cptr)smem + wid * K3_WAVE;
    const int l15 = lane & 15, g4 = lane >> 4;
    const char* A = ws + WS_OH;
    unsigned ao[4], wo_[5];
#pragma unroll
    for (int i = 0; i < 4; ++i) { const int row = 16 * i + (lane >> 2), ch = (lane & 3) ^ swz4(row); ao[i] = (unsigned)((mt * 64 + row) * KOH + ch * 8) * 2u; }
#pragma unroll
    for (int i = 0; i < 5; ++i) {
        const int U = 4 * i + (lane >> 4), o = U / 5, t = U % 5, hh = (lane >> 3) & 1, q = (lane & 7) >> 1, c = lane & 1;
        const int k = 8 * o + 4 * (hh ^ (o & 1)) + q;
        wo_[i] = (unsigned)(k * D + nt * 80 + 16 * t + 8 * c) * 2u;
    }
    auto issue = [&](int s) {
        const int ks = wid * K3_NS + s; const unsigned dst = lds0 + (s % K3_NST) * K3_STAGE;
        const char* ab = A + ks * 64;
        const char* wb = ks < 20 ? (const char*)wo + (size_t)ks * 32 * D * 2 : (const char*)wd + (size_t)(ks - 20) * 32 * D * 2;
#pragma unroll
        for (int i = 0; i < 4; ++i) glds16(ao[i], ab, dst + i * 1024);
#pragma unroll
        for (int i = 0; i < 5; ++i) glds16(wo_[i], wb, dst + 4096 + i * 1024);
    };
    const int ard = l15 * 64 + ((g4 ^ swz4(l15)) * 16);
    const int wrd1 = 4096 + (g4 * 5) * 256 + (g4 & 1) * 128 + (l15 >> 2) * 32 + (l15 & 3) * 8, wrd2 = wrd1 ^ 128;
    f32x4 acc[5][4];
#pragma unroll
    for (int a = 0; a < 5; ++a)
#pragma unroll
        for (int b = 0; b < 4; ++b) acc[a][b] = (f32x4){0.f, 0.f, 0.f, 0.f};
    issue(0); issue(1); issue(2);
#pragma unroll
    for (int s = 0; s < K3_NS; ++s) {
        if (s + 2 < K3_NS) wait_vm<18>(); else if (s + 1 < K3_NS) wait_vm<9>(); else wait_vm<0>();
        if (s + 3 < K3_NS) { asm volatile("s_waitcnt lgkmcnt(0)" ::: "memory"); issue(s + 3); }
        const lds_cptr base = L + (s % K3_NST) * K3_STAGE;
        bf16x8 af[4], wf[5];
#pragma unroll
        for (int t = 0; t < 4; ++t) af[t] = lds_b128(base + ard + t * 1024);
#pragma unroll
        for (int n = 0; n < 5; ++n) wf[n] = cat8(lds_tr(base + wrd1 + n * 256), lds_tr(base + wrd2 + n * 256));
#pragma unroll
        for (int n = 0; n < 5; ++n)
#pragma unroll
            for (int t = 0; t < 4; ++t) acc[n][t] = MFMA16(wf[n], af[t], acc[n][t]);
    }
    asm volatile("s_waitcnt lgkmcnt(0)" ::: "memory");
#pragma unroll
    for (int n = 0; n < 5; ++n)
#pragma unroll
        for (int t = 0; t < 4; ++t) *(LAS f32x4*)((lds_ptr)smem + wid * K3_WAVE + ((t * 5 + n) * 64 + lane) * 16) = acc[n][t];
    __syncthreads();
    const int tok = mt * 64 + wid * 16 + l15;
#pragma unroll
    for (int n = 0; n < 5; ++n) {
        f32x4 v = *(LAS const f32x4*)((lds_cptr)smem + ((wid * 5 + n) * 64 + lane) * 16);
#pragma unroll
        for (int w = 1; w < 4; ++w) v += *(LAS const f32x4*)((lds_cptr)smem + w * K3_WAVE + ((wid * 5 + n) * 64 + lane) * 16);
        *(u32x2*)(out + (size_t)tok * D + nt * 80 + n * 16 + g4 * 4) = (u32x2){cvtpk(v[0], v[1]), cvtpk(v[2], v[3])};
    }
    PROF_END(2);
}

extern "C" void kernel_launch(void* const* d_in, const int* in_sizes, int n_in, void* d_out, int out_size, void* d_ws, size_t ws_size, hipStream_t stream) {
    const u16* x = (const u16*)d_in[0]; const u16* wqkv = (const u16*)d_in[1]; const u16* wo = (const u16*)d_in[2];
    const u16* wg = (const u16*)d_in[4]; const u16* wu = (const u16*)d_in[5]; const u16* wd = (const u16*)d_in[6]; const u16* gw = (const u16*)d_in[7];
    char* ws = (char*)d_ws;
    hipFuncSetAttribute((const void*)k1_proj, hipFuncAttributeMaxDynamicSharedMemorySize, K1_LDS);
    hipFuncSetAttribute((const void*)k2_attn, hipFuncAttributeMaxDynamicSharedMemorySize, K2_LDS);
    hipFuncSetAttribute((const void*)k3_out, hipFuncAttributeMaxDynamicSharedMemorySize, K3_LDS);
    k1_proj<<<256, 512, K1_LDS, stream>>>(x, wqkv, wg, wu, gw, ws);
    k2_attn<<<256, 512, K2_LDS, stream>>>(ws);
    k3_out<<<256, 256, K3_LDS, stream>>>(wo, wd, ws, (u16*)d_out);
}
```

```cpp
#include <hip/hip_runtime.h>
#include <hip/hip_bf16.h>
#include <stdint.h>

typedef unsigned short u16;
typedef short bf16x8 __attribute__((ext_vector_type(8)));
typedef short s16x4 __attribute__((ext_vector_type(4)));
typedef float f32x4 __attribute__((ext_vector_type(4)));
typedef float f32x16 __attribute__((ext_vector_type(16)));
typedef unsigned u32x4 __attribute__((ext_vector_type(4)));
typedef unsigned u32x2 __attribute__((ext_vector_type(2)));
typedef float f32x2 __attribute__((ext_vector_type(2)));
typedef __bf16 bf16x2_t __attribute__((ext_vector_type(2)));
#define LAS __attribute__((address_space(3)))
typedef LAS const char* lds_cptr;
typedef LAS char* lds_ptr;

constexpr int S = 2048, D = 640, NH = 32, DH = 20, FF = 1536, NQKV = 1920;
constexpr float QSCALE = 0.22360679774997896f * 1.4426950408889634f;

constexpr size_t WS_Q = 0;
constexpr size_t WS_KV = WS_Q + (size_t)NH * S * DH * 2;
constexpr size_t WS_KMAX = WS_KV + (size_t)NH * 64 * 3072;
constexpr size_t WS_G = WS_KMAX + 160 * 32 * 4;
constexpr size_t WS_O = WS_G + S * 4;
constexpr size_t WS_H = WS_O + (size_t)S * D * 2;
constexpr size_t WS_END = WS_H + (size_t)S * FF * 2;

#define PROF_BEGIN()
#define PROF_END(kid)
#define PH_DECL()
#define PH_ARG
#define PH_PASS
#define PH(i)
#define PH_OUT(ws, kid)

#define STAMP(var)
#define STAMP_INIT()
#define STAMP_OUT(ws, kid)
#define WVB(N) wait_vm_bar<N>()
__device__ __forceinline__ unsigned cvtpk(float lo, float hi) { f32x2 v = {lo, hi}; bf16x2_t b = __builtin_convertvector(v, bf16x2_t); return __builtin_bit_cast(unsigned, b); }
__device__ __forceinline__ float bf_lo(unsigned u) { return __uint_as_float(u << 16); }
__device__ __forceinline__ float bf_hi(unsigned u) { return __uint_as_float(u & 0xffff0000u); }
__device__ __forceinline__ void glds16(unsigned voff, const void* sbase, unsigned lds_dst) {
    unsigned keep;
    asm volatile("s_mov_b32 %0, m0\n\ts_mov_b32 m0, %3\n\ts_nop 0\n\tglobal_load_lds_dwordx4 %1, %2\n\ts_mov_b32 m0, %0"
                 : "=&s"(keep) : "v"(voff), "s"(sbase), "s"(lds_dst) : "memory");
}
template <int N> __device__ __forceinline__ void wait_vm() { asm volatile("s_waitcnt vmcnt(%0)" :: "i"(N) : "memory"); }
template <int N> __device__ __forceinline__ void wait_vm_bar() { asm volatile("s_waitcnt vmcnt(%0) lgkmcnt(0)\n\ts_barrier" :: "i"(N) : "memory"); }
__device__ __forceinline__ void bar() { asm volatile("s_barrier" ::: "memory"); }
__device__ __forceinline__ void lgkm_bar() { asm volatile("s_waitcnt lgkmcnt(0)\n\ts_barrier" ::: "memory"); }
__device__ __forceinline__ bf16x8 lds_b128(lds_cptr p) { return *(LAS const bf16x8*)p; }
typedef short v4i16_t __attribute__((ext_vector_type(4)));
__device__ __forceinline__ s16x4 lds_tr(lds_cptr p) { return __builtin_bit_cast(s16x4, __builtin_amdgcn_ds_read_tr16_b64_v4i16((LAS v4i16_t*)p)); }
__device__ __forceinline__ bf16x8 cat8(s16x4 a, s16x4 b) { return (bf16x8){a[0], a[1], a[2], a[3], b[0], b[1], b[2], b[3]}; }
#define SBAR() __builtin_amdgcn_sched_barrier(0)
#define PIN(x) asm volatile("" : "+v"(x))
#define MFMA16(a, b, c) __builtin_amdgcn_mfma_f32_16x16x32_bf16(a, b, c, 0, 0, 0)
#define MFMA32(a, b, c) __builtin_amdgcn_mfma_f32_32x32x16_bf16(a, b, c, 0, 0, 0)
#define BC8(x) __builtin_bit_cast(bf16x8, x)

constexpr int K1_XSL = 16384, K1_NX = 3, K1_WSL = 20736, K1_NW = 5, K1_WOFF = K1_NX * K1_XSL;
constexpr int K1_WG = 8192, K1_WU = 8192 + 6144 + 32;
constexpr int K1_GW = K1_WOFF + K1_NW * K1_WSL;
constexpr int K1_GB = K1_GW + 2048;
constexpr int K1_LDS = K1_GB + 512;

__device__ __forceinline__ void k1_loader(const u16* __restrict__ x, const u16* __restrict__ wqkv, const u16* __restrict__ wg, const u16* __restrict__ wu,
                                          const u16* __restrict__ gatew, char* smem, int lane, int l, int rb, int j, int i0, char* wsp) {
    const unsigned lds0 = (unsigned)(uintptr_t)smem;
    const int l15 = lane & 15, g4 = lane >> 4, jc = j < 15 ? j : 14;
    unsigned xo[4], qo[2], go[3];
#pragma unroll
    for (int i = 0; i < 4; ++i) { const int row = 32 * l + 8 * i + (lane >> 3), ch = (lane & 7) ^ ((row >> 1) & 7); xo[i] = (unsigned)((rb * 128 + row) * D + ch * 8) * 2u; }
#pragma unroll
    for (int i = 0; i < 2; ++i) { const int row = 8 * l + 4 * i + g4, f = (row & 3) | (((row >> 3) & 1) << 2), ch = l15 ^ (f << 1); qo[i] = (unsigned)(row * NQKV + jc * 128 + ch * 8) * 2u; }
#pragma unroll
    for (int i = 0; i < 3; ++i) { const int p = ((l & 1) * 3 + i) * 64 + lane, row = p / 12, ch = p - row * 12; go[i] = (unsigned)(row * FF + j * 96 + ch * 8) * 2u; }
    const char* gsrc = (const char*)(l < 2 ? wg : wu);
    const unsigned gdst = lds0 + K1_WOFF + (l < 2 ? K1_WG : K1_WU) + (l & 1) * 3072, qdst = lds0 + K1_WOFF + l * 2048, xdst = lds0 + l * 4096;
    auto W = [&](int s) {
        int ii = (s >> 1) + i0; if (ii >= 10) ii -= 10; const int ks = 2 * ii + (s & 1); const unsigned so = (s % K1_NW) * K1_WSL;
        const char* qb = (const char*)wqkv + (size_t)ks * (32 * NQKV * 2); const char* gb = gsrc + (size_t)ks * (32 * FF * 2);
        glds16(qo[0], qb, qdst + so); glds16(qo[1], qb, qdst + so + 1024);
        glds16(go[0], gb, gdst + so); glds16(go[1], gb, gdst + so + 1024); glds16(go[2], gb, gdst + so + 2048);
    };
    auto X = [&](int i) {
        int ii = i + i0; if (ii >= 10) ii -= 10; const char* xb = (const char*)x + ii * 128; const unsigned so = (i % K1_NX) * K1_XSL;
#pragma unroll
        for (int q = 0; q < 4; ++q) glds16(xo[q], xb, xdst + so + q * 1024);
    };
    if (l == 0) { glds16(lane * 16u, gatew, lds0 + K1_GW); glds16((64u + (lane < 16 ? lane : 15)) * 16u, gatew, lds0 + K1_GW + 1024); }
    STAMP_INIT();
    X(0); W(0); W(1); W(2);
    WVB(10);
    W(3); X(1);
    WVB(14); W(4); X(2);
    WVB(9); W(5);
    for (int s = 2; s < 16; s += 2) {
        WVB(14); W(s + 4); if (s / 2 + 2 < 10) X(s / 2 + 2); else { asm volatile("" ::: "memory"); }
        WVB(14); if (s + 5 < 20) W(s + 5);
    }
    WVB(14);
    WVB(5);
    WVB(0);
    STAMP_OUT(wsp, 0);
    lgkm_bar();
    lgkm_bar();
}

template <bool VT>
__device__ __forceinline__ void k1_qkv(char* __restrict__ ws, char* smem, int lane, int wid, int rb, int j, int i0 PH_ARG) {
    const lds_cptr L = (lds_cptr)smem;
    const int l15 = lane & 15, g4 = lane >> 4, wm = wid >> 1, wn = wid & 1;
    const int xrd = (wm * 64 + l15) * 128 + ((g4 ^ ((l15 >> 1) & 7)) << 4);
    int wrd[4];
#pragma unroll
    for (int n = 0; n < 4; ++n) { const int q = l15 >> 2, p = l15 & 3, ch = (2 * (wn * 4 + n) + (p >> 1)) ^ (2 * q | ((g4 & 1) << 3)); wrd[n] = K1_WOFF + (8 * g4 + q) * 256 + ch * 16 + (p & 1) * 8; }
    f32x4 acc[4][4], gacc[2];
#pragma unroll
    for (int a = 0; a < 4; ++a)
#pragma unroll
        for (int b = 0; b < 4; ++b) acc[a][b] = (f32x4){0.f, 0.f, 0.f, 0.f};
    gacc[0] = gacc[1] = (f32x4){0.f, 0.f, 0.f, 0.f};
    bf16x8 xf[2][4], wf[2][4], gf[2];
    int xo_ = 0, wo_ = 0, go_ = 0;
    lds_cptr xb_, wb_, gb_;
    auto rdcur = [&](int half) { xb_ = L + xo_ + (xrd ^ (half << 6)); wb_ = L + wo_; gb_ = L + K1_GW + go_ + g4 * 16; };
    auto adv = [&](int half) {
        wo_ += K1_WSL; if (wo_ == K1_NW * K1_WSL) wo_ = 0;
        go_ += 64; if (go_ == 1280) go_ = 0;
        if (half) { xo_ += K1_XSL; if (xo_ == K1_NX * K1_XSL) xo_ = 0; } };
    auto rdx = [&](int b, int t) { xf[b][t] = lds_b128(xb_ + t * 2048); };
    auto rdw = [&](int b, int n) { wf[b][n] = cat8(lds_tr(wb_ + wrd[n]), lds_tr(wb_ + wrd[n] + 1024)); };
    auto rdg = [&](int b) { gf[b] = lds_b128(gb_); };
    auto mmn = [&](int b, int n) {
#pragma unroll
        for (int t = 0; t < 4; ++t) acc[n][t] = VT ? MFMA16(xf[b][t], wf[b][n], acc[n][t]) : MFMA16(wf[b][n], xf[b][t], acc[n][t]);
    };
    auto mmg = [&](int b) { if (wn) { gacc[0] = MFMA16(gf[b], xf[b][2], gacc[0]); gacc[1] = MFMA16(gf[b], xf[b][3], gacc[1]); }
                            else    { gacc[0] = MFMA16(gf[b], xf[b][0], gacc[0]); gacc[1] = MFMA16(gf[b], xf[b][1], gacc[1]); } };
    PH(1);
    lgkm_bar();
    PH(2);
    go_ = i0 * 128;
    rdcur(0);
#pragma unroll
    for (int t = 0; t < 4; ++t) { rdx(0, t); rdw(0, t); }
    rdg(0);
    adv(0); rdcur(1);
#define QKV_STEP(B) do { bar(); SBAR(); \
        mmn(B, 0); SBAR(); rdx(B ^ 1, 0); rdx(B ^ 1, 1); rdw(B ^ 1, 0); SBAR(); \
        mmn(B, 1); SBAR(); rdx(B ^ 1, 2); rdx(B ^ 1, 3); rdw(B ^ 1, 1); SBAR(); \
        mmn(B, 2); SBAR(); rdw(B ^ 1, 2); rdg(B ^ 1); SBAR(); \
        mmg(B); SBAR(); \
        mmn(B, 3); SBAR(); rdw(B ^ 1, 3); adv(B ^ 1); rdcur(B); SBAR(); } while (0)
    __builtin_amdgcn_s_waitcnt(0xC07F);
    for (int i = 0; i < 10; ++i) {
        QKV_STEP(0);
        QKV_STEP(1);
    }
#undef QKV_STEP
    PH(3);
#pragma unroll
    for (int t = 0; t < 2; ++t) {
        const float gg = __builtin_amdgcn_rcpf(1.f + __builtin_amdgcn_exp2f(-1.4426950408889634f * gacc[t][0]));
        if (g4 == 0) { const int tok = wm * 64 + (wn * 2 + t) * 16 + l15; *(LAS float*)((lds_ptr)smem + K1_GB + tok * 4) = gg;
                       if (j == 0) ((float*)(ws + WS_G))[rb * 128 + tok] = gg; }
    }
    lgkm_bar();
    PH(4);
    if (j >= 15) return;
    char* Qs = ws + WS_Q; char* KV = ws + WS_KV;
    if constexpr (!VT) {
        const bool isK = j >= 5;
#pragma unroll
        for (int n = 0; n < 4; ++n) {
            const int u = (isK ? j - 5 : j) * 32 + wn * 16 + n * 4 + g4;
            const int head = u / 5, jj = u - head * 5;
            float mx = 0.f;
#pragma unroll
            for (int t = 0; t < 4; ++t) {
                const int tok = rb * 128 + wm * 64 + t * 16 + l15; const f32x4 v = acc[n][t];
                if (isK) {
                    const u32x2 w = {cvtpk(v[0], v[1]), cvtpk(v[2], v[3])};
                    mx = fmaxf(mx, v[0] * v[0] + v[1] * v[1] + v[2] * v[2] + v[3] * v[3]);
                    char* tb = KV + ((size_t)head * 64 + (tok >> 5)) * 3072; const int key = tok & 31;
                    if (jj < 4) *(u32x2*)(tb + (jj >> 1) * 512 + key * 16 + (jj & 1) * 8) = w;
                    else *(u32x4*)(tb + 1024 + key * 16) = (u32x4){w[0], w[1], 0x3f803f80u, 0u};
                } else {
                    *(u32x2*)(Qs + ((size_t)u * S + tok) * 8) = (u32x2){cvtpk(v[0] * QSCALE, v[1] * QSCALE), cvtpk(v[2] * QSCALE, v[3] * QSCALE)};
                }
            }
            if (isK) {
                mx = fmaxf(mx, __shfl_xor(mx, 1)); mx = fmaxf(mx, __shfl_xor(mx, 2)); mx = fmaxf(mx, __shfl_xor(mx, 4)); mx = fmaxf(mx, __shfl_xor(mx, 8));
                if (l15 == 0) ((float*)(ws + WS_KMAX))[u * 32 + rb * 2 + wm] = mx;
            }
        }
    } else {
#pragma unroll
        for (int n = 0; n < 4; ++n) {
            const int c = (j - 10) * 128 + wn * 64 + n * 16 + l15; const int head = c / 20, row = c - head * 20;
#pragma unroll
            for (int t = 0; t < 4; ++t) {
                const int tok0 = rb * 128 + wm * 64 + t * 16; const f32x4 v = acc[n][t];
                char* tb = KV + ((size_t)head * 64 + (tok0 >> 5)) * 3072 + 1536 + ((((tok0 >> 4) & 1) * 2 + (g4 & 1)) * 24) * 16 + (g4 >> 1) * 8;
                *(u32x2*)(tb + row * 16) = (u32x2){cvtpk(v[0], v[1]), cvtpk(v[2], v[3])};
                if (row < 4) { const unsigned cc = row == 0 ? 0x3f803f80u : 0u; *(u32x2*)(tb + (20 + row) * 16) = (u32x2){cc, cc}; }
            }
        }
    }
}

__device__ __forceinline__ void k1_gu(char* __restrict__ ws, char* smem, int lane, int w4, int rb, int j PH_ARG) {
    const lds_cptr L = (lds_cptr)smem;
    const int l15 = lane & 15, g4 = lane >> 4, l31 = lane & 31, hi = lane >> 5, wm = w4 >> 1, wn = w4 & 1;
    const int xrd = (wm * 64 + l31) * 128 + ((hi ^ ((l31 >> 1) & 7)) << 4);
    const int wrd = K1_WOFF + K1_WG + (g4 & 1) * (K1_WU - K1_WG) + (8 * hi + (l15 >> 2)) * 192 + wn * 96 + (l15 & 3) * 8;
    f32x16 acc[3][2];
#pragma unroll
    for (int a = 0; a < 3; ++a)
#pragma unroll
        for (int b = 0; b < 2; ++b) acc[a][b] = (f32x16){};
    bf16x8 xf[2][2][2], wf[2][2][3];
    int xo_ = 0, wo_ = 0;
    lds_cptr xb_, wb_;
    auto rdcur = [&]() { xb_ = L + xo_; wb_ = L + wrd + wo_; };
    auto adv = [&](int half) { wo_ += K1_WSL; if (wo_ == K1_NW * K1_WSL) wo_ = 0; if (half) { xo_ += K1_XSL; if (xo_ == K1_NX * K1_XSL) xo_ = 0; } };
    auto rdx = [&](int b, int ks, int t) { xf[b][ks][t] = lds_b128(xb_ + (xrd ^ ((b << 6) | (ks << 5))) + t * 4096); };
    auto rdw = [&](int b, int ks, int n) { wf[b][ks][n] = cat8(lds_tr(wb_ + ks * 3072 + n * 32), lds_tr(wb_ + ks * 3072 + 768 + n * 32)); };
    auto mmk = [&](int b, int ks, int n) {
#pragma unroll
        for (int t = 0; t < 2; ++t) acc[n][t] = MFMA32(wf[b][ks][n], xf[b][ks][t], acc[n][t]);
    };
    PH(1);
    lgkm_bar();
    PH(2);
    rdcur();
#pragma unroll
    for (int ks = 0; ks < 2; ++ks) { rdx(0, ks, 0); rdx(0, ks, 1); rdw(0, ks, 0); rdw(0, ks, 1); rdw(0, ks, 2); }
    adv(0); rdcur();
#define GU_STEP(B) do { bar(); SBAR(); \
        mmk(B, 0, 0); SBAR(); rdw(B ^ 1, 0, 0); SBAR(); \
        mmk(B, 0, 1); SBAR(); rdw(B ^ 1, 0, 1); SBAR(); \
        mmk(B, 0, 2); SBAR(); rdw(B ^ 1, 0, 2); rdx(B ^ 1, 0, 0); rdx(B ^ 1, 0, 1); SBAR(); \
        mmk(B, 1, 0); SBAR(); rdw(B ^ 1, 1, 0); SBAR(); \
        mmk(B, 1, 1); SBAR(); rdw(B ^ 1, 1, 1); SBAR(); \
        mmk(B, 1, 2); SBAR(); rdw(B ^ 1, 1, 2); rdx(B ^ 1, 1, 0); rdx(B ^ 1, 1, 1); adv(B ^ 1); rdcur(); SBAR(); } while (0)
    __builtin_amdgcn_s_waitcnt(0xC07F);
    for (int i = 0; i < 10; ++i) {
        GU_STEP(0);
        GU_STEP(1);
    }
#undef GU_STEP
    PH(3);
    lgkm_bar();
    PH(4);
    char* Hh = ws + WS_H;
#pragma unroll
    for (int t = 0; t < 2; ++t) {
        const int tl = wm * 64 + t * 32 + l31; const float omg = 1.f - *(LAS const float*)(L + K1_GB + tl * 4);
#pragma unroll
        for (int n = 0; n < 3; ++n) {
            unsigned w[4];
#pragma unroll
            for (int hf = 0; hf < 2; ++hf) {
                float h[4];
#pragma unroll
                for (int e = 0; e < 4; ++e) {
                    const float gt = acc[n][t][4 * hf + e], up = acc[n][t][8 + 4 * hf + e];
                    const float u = gt * (1.f + 0.044715f * gt * gt) * (-2.f * 0.7978845608028654f * 1.4426950408889634f);
                    h[e] = gt * __builtin_amdgcn_rcpf(1.f + __builtin_amdgcn_exp2f(u)) * up * omg;
                }
                w[2 * hf] = cvtpk(h[0], h[1]); w[2 * hf + 1] = cvtpk(h[2], h[3]);
            }
            const auto r0 = __builtin_amdgcn_permlane32_swap(w[0], w[2], false, false), r1 = __builtin_amdgcn_permlane32_swap(w[1], w[3], false, false);
            *(u32x4*)(Hh + ((size_t)(j * 6 + wn * 3 + n) * S + rb * 128 + tl) * 32 + hi * 16) = (u32x4){r0[0], r1[0], r0[1], r1[1]};
        }
    }
}

__global__ __launch_bounds__(768) void k1_proj(const u16* __restrict__ x, const u16* __restrict__ wqkv, const u16* __restrict__ wg,
                                               const u16* __restrict__ wu, const u16* __restrict__ gatew, char* __restrict__ ws) {
    extern __shared__ __attribute__((aligned(16))) char smem[];
    const int tid = threadIdx.x, lane = tid & 63, wid = __builtin_amdgcn_readfirstlane(tid >> 6);
    PROF_BEGIN(); char* ws_prof = ws; PH_DECL(); PH(0);
    const int bid = blockIdx.x, xcd = bid & 7, bi = bid >> 3;
    const int rb = (xcd & 1) * 8 + (bi & 7), j = (xcd >> 1) * 4 + (bi >> 3);
    const int i0 = 0;
    if (wid >= 8) k1_loader(x, wqkv, wg, wu, gatew, smem, lane, wid - 8, rb, j, i0, ws);
    else if (wid < 4) { if (j >= 10) k1_qkv<true>(ws, smem, lane, wid, rb, j, i0 PH_PASS); else k1_qkv<false>(ws, smem, lane, wid, rb, j, i0 PH_PASS); }
    else k1_gu(ws, smem, lane, wid - 4, rb, j PH_PASS);
    PH(5);
    PH(6); PH_OUT(ws, 0);
    PROF_END(0);
}

constexpr int K2_SL = 8 * 3072, K2_NSL = 4, K2_FLAG = K2_NSL * K2_SL, K2_LDS = K2_FLAG + 64;
constexpr float L_MIN = 5.421010862427522e-20f;

__device__ __forceinline__ void attn_issue(const char* kvh, unsigned lds0, int wid, unsigned voff, int c) {
    const char* src = kvh + (size_t)((wid >> 2) * 32 + 4 * c) * 3072 + (wid & 3) * 3072; const unsigned dst = lds0 + (c % K2_NSL) * K2_SL + wid * 3072;
    glds16(voff, src, dst); glds16(voff, src + 1024, dst + 1024); glds16(voff, src + 2048, dst + 2048);
}
__device__ __forceinline__ void attn_maxpass(const char* kvh, unsigned lds0, lds_cptr L, int wid, int lane, const bf16x8 (&q0)[2], const bf16x8 (&q1)[2], float (&mx)[2]) {
    const int l31 = lane & 31, hi = lane >> 5; const unsigned voff = (unsigned)(lane * 16);
    const int k0o = (wid >> 2) * 12288 + hi * 512 + l31 * 16, k1o = (wid >> 2) * 12288 + 1024 + l31 * 16;
    mx[0] = mx[1] = -INFINITY;
    for (int c = 0; c < 8; ++c) {
        attn_issue(kvh, lds0, wid, voff, c);
        wait_vm_bar<0>();
        const lds_cptr base = L + (c % K2_NSL) * K2_SL;
        for (int t = 0; t < 4; ++t) {
            const bf16x8 kf0 = lds_b128(base + t * 3072 + k0o), kf1 = lds_b128(base + t * 3072 + k1o);
#pragma unroll
            for (int z = 0; z < 2; ++z) {
                f32x16 s = MFMA32(kf0, q0[z], (f32x16){}); s = MFMA32(kf1, q1[z], s);
#pragma unroll
                for (int r = 0; r < 16; ++r) mx[z] = fmaxf(mx[z], s[r]);
            }
        }
        lgkm_bar();
    }
}
__device__ __forceinline__ void attn_stream(const char* kvh, unsigned lds0, lds_cptr L, int wid, int lane, const bf16x8 (&q0)[2], const bf16x8 (&q1)[2], f32x16& oa, f32x16& ob, bool first) {
    const int l31 = lane & 31, hi = lane >> 5;
    const unsigned voff = (unsigned)(lane * 16);
    const int hb = (wid >> 2) * 12288;
    const int k0o = hb + hi * 512 + l31 * 16, k1o = hb + 1024 + l31 * 16, vo = hb + 1536 + hi * 384 + l31 * 16;
    if (!first) { attn_issue(kvh, lds0, wid, voff, 0); attn_issue(kvh, lds0, wid, voff, 1); attn_issue(kvh, lds0, wid, voff, 2); }
    wait_vm_bar<6>();
    f32x16 sa[2], sb[2]; u32x4 pa0, pa1 = {0u, 0u, 0u, 0u}, pb0, pb1 = {0u, 0u, 0u, 0u}; bf16x8 kf0, kf1, vf1 = {0, 0, 0, 0, 0, 0, 0, 0};
    { const bf16x8 a = lds_b128(L + k0o), b = lds_b128(L + k1o);
      sa[0] = MFMA32(a, q0[0], (f32x16){}); sa[0] = MFMA32(b, q1[0], sa[0]); sb[0] = MFMA32(a, q0[1], (f32x16){}); sb[0] = MFMA32(b, q1[1], sb[0]); }
    kf0 = lds_b128(L + 3072 + k0o); kf1 = lds_b128(L + 3072 + k1o);
#define EXP2(SC, i) cvtpk(__builtin_amdgcn_exp2f(SC[i]), __builtin_amdgcn_exp2f(SC[(i) + 1]))
#define TILE(C, N, VB, KN) do { \
        const bf16x8 nv0 = lds_b128((VB) + vo); SBAR(); \
        oa = MFMA32(vf1, BC8(pa1), oa); pa0[0] = EXP2(sa[C], 0); pa0[1] = EXP2(sa[C], 2); PIN(pa0); SBAR(); \
        ob = MFMA32(vf1, BC8(pb1), ob); pa0[2] = EXP2(sa[C], 4); pa0[3] = EXP2(sa[C], 6); PIN(pa0); SBAR(); \
        vf1 = lds_b128((VB) + vo + 768); SBAR(); \
        sa[N] = MFMA32(kf0, q0[0], (f32x16){}); pa1[0] = EXP2(sa[C], 8); pa1[1] = EXP2(sa[C], 10); PIN(pa1); SBAR(); \
        sa[N] = MFMA32(kf1, q1[0], sa[N]); pa1[2] = EXP2(sa[C], 12); pa1[3] = EXP2(sa[C], 14); PIN(pa1); SBAR(); \
        oa = MFMA32(nv0, BC8(pa0), oa); pb0[0] = EXP2(sb[C], 0); pb0[1] = EXP2(sb[C], 2); PIN(pb0); SBAR(); \
        sb[N] = MFMA32(kf0, q0[1], (f32x16){}); pb0[2] = EXP2(sb[C], 4); pb0[3] = EXP2(sb[C], 6); PIN(pb0); SBAR(); \
        kf0 = lds_b128((KN) + k0o); SBAR(); \
        sb[N] = MFMA32(kf1, q1[1], sb[N]); pb1[0] = EXP2(sb[C], 8); pb1[1] = EXP2(sb[C], 10); PIN(pb1); SBAR(); \
        kf1 = lds_b128((KN) + k1o); SBAR(); \
        ob = MFMA32(nv0, BC8(pb0), ob); pb1[2] = EXP2(sb[C], 12); pb1[3] = EXP2(sb[C], 14); PIN(pb1); SBAR(); \
    } while (0)
    for (int c = 0; c < 8; ++c) {
        const lds_cptr b0 = L + (c % K2_NSL) * K2_SL, b1 = L + ((c + 1) % K2_NSL) * K2_SL;
        TILE(0, 1, b0 + 0 * 3072, b0 + 2 * 3072);
        TILE(1, 0, b0 + 1 * 3072, b0 + 3 * 3072);
        if (c < 6) wait_vm_bar<3>(); else wait_vm_bar<0>();
        if (c + 3 < 8) attn_issue(kvh, lds0, wid, voff, c + 3);
        TILE(0, 1, b0 + 2 * 3072, b1);
        TILE(1, 0, b0 + 3 * 3072, b1 + 3072);
    }
    oa = MFMA32(vf1, BC8(pa1), oa); ob = MFMA32(vf1, BC8(pb1), ob);
    lgkm_bar();
#undef TILE
#undef EXP2
}
__device__ __forceinline__ unsigned neg_split(float m) {
    const float mh = __uint_as_float(__float_as_uint(m) & 0xffff0000u); const float ml = m - mh;
    return (__float_as_uint(-mh) >> 16) | (cvtpk(-ml, 0.f) << 16);
}

__global__ __launch_bounds__(512, 2) void k2_attn(char* __restrict__ ws) {
    extern __shared__ __attribute__((aligned(16))) char smem[];
    const int tid = threadIdx.x, lane = tid & 63, wid = __builtin_amdgcn_readfirstlane(tid >> 6);
    PROF_BEGIN(); char* ws_prof = ws; PH_DECL(); PH(0);
    const int bid = blockIdx.x, head = (bid & 7) * 4 + ((bid >> 3) & 3), qb = bid >> 5;
    const unsigned lds0 = (unsigned)(uintptr_t)smem; const lds_cptr L = (lds_cptr)smem;
    const int l31 = lane & 31, hi = lane >> 5;
    const char* kvh = ws + WS_KV + (size_t)head * 64 * 3072;
    { const unsigned voff = (unsigned)(lane * 16); attn_issue(kvh, lds0, wid, voff, 0); attn_issue(kvh, lds0, wid, voff, 1); attn_issue(kvh, lds0, wid, voff, 2); }
    const int tok0 = qb * 256 + (wid & 3) * 64 + l31;
    u32x2 qa[2], qc[2], qe[2]; float gq[2], km[5];
#pragma unroll
    for (int z = 0; z < 2; ++z) {
        const char* qp = ws + WS_Q + ((size_t)head * 5 * S + tok0 + 32 * z) * 8;
        qa[z] = *(const u32x2*)(qp + (size_t)(2 * hi) * S * 8); qc[z] = *(const u32x2*)(qp + (size_t)(2 * hi + 1) * S * 8); qe[z] = *(const u32x2*)(qp + (size_t)4 * S * 8);
        gq[z] = ((const float*)(ws + WS_G))[tok0 + 32 * z];
    }
#pragma unroll
    for (int u = 0; u < 5; ++u) km[u] = ((const float*)(ws + WS_KMAX))[(head * 5 + u) * 32 + l31];
    float kmx = 0.f;
#pragma unroll
    for (int u = 0; u < 5; ++u) { float v = km[u];
#pragma unroll
        for (int o = 1; o < 32; o <<= 1) v = fmaxf(v, __shfl_xor(v, o));
        kmx += v; }
    float m[2]; bf16x8 q0[2];
#pragma unroll
    for (int z = 0; z < 2; ++z) {
        float qn = bf_lo(qa[z][0]) * bf_lo(qa[z][0]) + bf_hi(qa[z][0]) * bf_hi(qa[z][0]) + bf_lo(qa[z][1]) * bf_lo(qa[z][1]) + bf_hi(qa[z][1]) * bf_hi(qa[z][1])
                 + bf_lo(qc[z][0]) * bf_lo(qc[z][0]) + bf_hi(qc[z][0]) * bf_hi(qc[z][0]) + bf_lo(qc[z][1]) * bf_lo(qc[z][1]) + bf_hi(qc[z][1]) * bf_hi(qc[z][1]);
        if (hi == 0) qn += bf_lo(qe[z][0]) * bf_lo(qe[z][0]) + bf_hi(qe[z][0]) * bf_hi(qe[z][0]) + bf_lo(qe[z][1]) * bf_lo(qe[z][1]) + bf_hi(qe[z][1]) * bf_hi(qe[z][1]);
        qn += __shfl_xor(qn, 32);
        m[z] = sqrtf(qn * kmx) * 1.02f + 1e-30f;
        q0[z] = BC8(((u32x4){qa[z][0], qa[z][1], qc[z][0], qc[z][1]}));
    }
    PH(1);
    f32x16 oa, ob; float l[2];
    LAS float* red = (LAS float*)smem;
    for (int attempt = 0;; ++attempt) {
        bf16x8 q1[2];
#pragma unroll
        for (int z = 0; z < 2; ++z) q1[z] = BC8((hi == 0 ? (u32x4){qe[z][0], qe[z][1], neg_split(m[z]), 0u} : (u32x4){0u, 0u, 0u, 0u}));
        oa = (f32x16){}; ob = (f32x16){};
        attn_stream(kvh, lds0, L, wid, lane, q0, q1, oa, ob, attempt == 0);
        PH(2);
#pragma unroll
        for (int r = 0; r < 12; ++r) { red[(wid * 24 + r) * 64 + lane] = oa[r]; red[(wid * 24 + 12 + r) * 64 + lane] = ob[r]; }
        lgkm_bar();
#pragma unroll
        for (int r = 0; r < 12; ++r) { oa[r] += red[((wid ^ 4) * 24 + r) * 64 + lane]; ob[r] += red[((wid ^ 4) * 24 + 12 + r) * 64 + lane]; }
        l[0] = __shfl(oa[8], 32 + l31); l[1] = __shfl(ob[8], 32 + l31);
        const bool bad = !(l[0] >= L_MIN) || !(l[1] >= L_MIN);
        if (attempt == 1) break;
        LAS unsigned* flag = (LAS unsigned*)((lds_ptr)smem + K2_FLAG);
        if (tid == 0) *flag = 0u;
        lgkm_bar();
        if (__any(bad) && lane == 0) *flag = 1u;
        lgkm_bar();
        const unsigned f = *(volatile LAS unsigned*)flag;
        lgkm_bar();
        if (!f) break;
        bf16x8 q1z[2];
#pragma unroll
        for (int z = 0; z < 2; ++z) q1z[z] = BC8((hi == 0 ? (u32x4){qe[z][0], qe[z][1], 0u, 0u} : (u32x4){0u, 0u, 0u, 0u}));
        float mx[2];
        attn_maxpass(kvh, lds0, L, wid, lane, q0, q1z, mx);
#pragma unroll
        for (int z = 0; z < 2; ++z) { mx[z] = fmaxf(mx[z], __shfl_xor(mx[z], 32)); red[(wid * 2 + z) * 64 + lane] = mx[z]; }
        lgkm_bar();
#pragma unroll
        for (int z = 0; z < 2; ++z) m[z] = fmaxf(mx[z], red[((wid ^ 4) * 2 + z) * 64 + lane]);
        lgkm_bar();
    }
    PH(3);
    {
        const int z = wid >> 2; const f32x16 o = z ? ob : oa;
        const float sc = (z ? gq[1] : gq[0]) * __builtin_amdgcn_rcpf(z ? l[1] : l[0]);
        const int tok = tok0 + 32 * z;
#pragma unroll
        for (int b = 0; b < 3; ++b) {
            if (b < 2 || hi == 0) {
                const int c = head * DH + b * 8 + hi * 4;
                *(u32x2*)(ws + WS_O + ((size_t)(c >> 4) * S + tok) * 32 + (c & 15) * 2) = (u32x2){cvtpk(o[4 * b] * sc, o[4 * b + 1] * sc), cvtpk(o[4 * b + 2] * sc, o[4 * b + 3] * sc)};
            }
        }
    }
    PH(4); PH_OUT(ws, 1);
    PROF_END(1);
}

constexpr int K3_STAGE = 9 * 1024, K3_NST = 4, K3_WAVE = K3_STAGE * K3_NST, K3_LDS = 4 * K3_WAVE, K3_NS = 17;

__global__ __launch_bounds__(256, 1) void k3_out(const u16* __restrict__ wo, const u16* __restrict__ wd, const char* __restrict__ ws, u16* __restrict__ out) {
    extern __shared__ __attribute__((aligned(16))) char smem[];
    const int tid = threadIdx.x, lane = tid & 63, wid = __builtin_amdgcn_readfirstlane(tid >> 6);
    PROF_BEGIN(); char* ws_prof = (char*)ws; PH_DECL(); PH(0);
    const int bid = blockIdx.x, xcd = bid & 7, bi = bid >> 3;
    const int mt = (xcd >> 1) * 8 + (bi & 7), nt = (xcd & 1) * 4 + (bi >> 3);
    const unsigned lds0 = (unsigned)(uintptr_t)smem + wid * K3_WAVE; const lds_cptr L = (lds_cptr)smem + wid * K3_WAVE;
    const int l15 = lane & 15, g4 = lane >> 4;
    const char* A = ws + WS_O + (size_t)mt * 64 * 32;
    const unsigned ao = (unsigned)lane * 16u;
    unsigned wo_[5];
#pragma unroll
    for (int i = 0; i < 5; ++i) { const int p = 64 * i + lane, row = p / 10, ch = p - row * 10; wo_[i] = (unsigned)(row * D + nt * 80 + ch * 8) * 2u; }
    auto issue = [&](int s) {
        const int ks = wid * K3_NS + s; const unsigned dst = lds0 + (s % K3_NST) * K3_STAGE;
        const char* ab = A + (size_t)(2 * ks) * S * 32;
        const char* wb = ks < 20 ? (const char*)wo + (size_t)ks * 32 * D * 2 : (const char*)wd + (size_t)(ks - 20) * 32 * D * 2;
        glds16(ao, ab, dst); glds16(ao, ab + 1024, dst + 1024); glds16(ao, ab + (size_t)S * 32, dst + 2048); glds16(ao, ab + (size_t)S * 32 + 1024, dst + 3072);
#pragma unroll
        for (int i = 0; i < 5; ++i) glds16(wo_[i], wb, dst + 4096 + i * 1024);
    };
    const int ard = (g4 >> 1) * 2048 + l15 * 32 + (g4 & 1) * 16;
    const int wrd = 4096 + (8 * g4 + (l15 >> 2)) * 160 + (l15 & 3) * 8;
    f32x4 acc[5][4];
#pragma unroll
    for (int a = 0; a < 5; ++a)
#pragma unroll
        for (int b = 0; b < 4; ++b) acc[a][b] = (f32x4){0.f, 0.f, 0.f, 0.f};
    issue(0); issue(1); issue(2);
    PH(1);
    for (int s = 0; s < K3_NS; ++s) {
        if (s + 2 < K3_NS) wait_vm<18>(); else if (s + 1 < K3_NS) wait_vm<9>(); else wait_vm<0>();
        const lds_cptr base = L + (s % K3_NST) * K3_STAGE;
        bf16x8 af[4], wf[5];
#pragma unroll
        for (int t = 0; t < 4; ++t) af[t] = lds_b128(base + ard + t * 512);
#pragma unroll
        for (int n = 0; n < 5; ++n) wf[n] = cat8(lds_tr(base + wrd + n * 32), lds_tr(base + wrd + 640 + n * 32));
        SBAR();
        if (s + 3 < K3_NS) issue(s + 3);
        SBAR();
#pragma unroll
        for (int n = 0; n < 5; ++n)
#pragma unroll
            for (int t = 0; t < 4; ++t) acc[n][t] = MFMA16(wf[n], af[t], acc[n][t]);
    }
    PH(2);
    asm volatile("s_waitcnt lgkmcnt(0)" ::: "memory");
#pragma unroll
    for (int n = 0; n < 5; ++n)
#pragma unroll
        for (int t = 0; t < 4; ++t) *(LAS f32x4*)((lds_ptr)smem + wid * K3_WAVE + ((t * 5 + n) * 64 + lane) * 16) = acc[n][t];
    __syncthreads();
    const int tok = mt * 64 + wid * 16 + l15;
#pragma unroll
    for (int n = 0; n < 5; ++n) {
        f32x4 v = *(LAS const f32x4*)((lds_cptr)smem + ((wid * 5 + n) * 64 + lane) * 16);
#pragma unroll
        for (int w = 1; w < 4; ++w) v += *(LAS const f32x4*)((lds_cptr)smem + w * K3_WAVE + ((wid * 5 + n) * 64 + lane) * 16);
        *(u32x2*)(out + (size_t)tok * D + nt * 80 + n * 16 + g4 * 4) = (u32x2){cvtpk(v[0], v[1]), cvtpk(v[2], v[3])};
    }
    PH(3); PH_OUT(ws, 2);
    PROF_END(2);
}

extern "C" void kernel_launch(void* const* d_in, const int* in_sizes, int n_in, void* d_out, int out_size, void* d_ws, size_t ws_size, hipStream_t stream) {
    const u16* x = (const u16*)d_in[0]; const u16* wqkv = (const u16*)d_in[1]; const u16* wo = (const u16*)d_in[2];
    const u16* wg = (const u16*)d_in[4]; const u16* wu = (const u16*)d_in[5]; const u16* wd = (const u16*)d_in[6]; const u16* gw = (const u16*)d_in[7];
    char* ws = (char*)d_ws;
    (void)hipFuncSetAttribute((const void*)k1_proj, hipFuncAttributeMaxDynamicSharedMemorySize, K1_LDS);
    (void)hipFuncSetAttribute((const void*)k2_attn, hipFuncAttributeMaxDynamicSharedMemorySize, K2_LDS);
    (void)hipFuncSetAttribute((const void*)k3_out, hipFuncAttributeMaxDynamicSharedMemorySize, K3_LDS);
    k1_proj<<<256, 768, K1_LDS, stream>>>(x, wqkv, wg, wu, gw, ws);
    k2_attn<<<256, 512, K2_LDS, stream>>>(ws);
    k3_out<<<256, 256, K3_LDS, stream>>>(wo, wd, ws, (u16*)d_out);
}
```

```cpp
#include <hip/hip_runtime.h>
#include <hip/hip_bf16.h>
#include <stdint.h>

typedef unsigned short u16;
typedef unsigned long long u64;
typedef short bf16x8 __attribute__((ext_vector_type(8)));
typedef short s16x4 __attribute__((ext_vector_type(4)));
typedef float f32x4 __attribute__((ext_vector_type(4)));
typedef float f32x16 __attribute__((ext_vector_type(16)));
typedef unsigned u32x4 __attribute__((ext_vector_type(4)));
typedef unsigned u32x2 __attribute__((ext_vector_type(2)));
typedef float f32x2 __attribute__((ext_vector_type(2)));
typedef __bf16 bf16x2_t __attribute__((ext_vector_type(2)));
#define LAS __attribute__((address_space(3)))
#define GAS __attribute__((address_space(1)))
typedef LAS const char* lds_cptr;
typedef LAS char* lds_ptr;
typedef __amdgpu_buffer_rsrc_t rsrc_t;

constexpr int S = 2048, D = 640, NH = 32, DH = 20, FF = 1536, NQKV = 1920;
constexpr float QSCALE = 0.22360679774997896f * 1.4426950408889634f;

constexpr unsigned WS_EPOCH = 0;
constexpr unsigned WS_FLQ = 1024;
constexpr unsigned WS_FLH = WS_FLQ + 2048;
constexpr unsigned WS_FLO = WS_FLH + 2048;
constexpr unsigned WS_Q = WS_FLO + 8 * 8 * 32 * 8 + 3072;
constexpr unsigned WS_KV = WS_Q + NH * 64 * 1536;
constexpr unsigned WS_KMAX = WS_KV + NH * 64 * 3072;
constexpr unsigned WS_G = WS_KMAX + 5 * 32 * 32 * 4;
constexpr unsigned WS_O = WS_G + S * 4;
constexpr unsigned WS_H = WS_O + S * D * 2;
constexpr unsigned WS_END = WS_H + S * FF * 2;
constexpr int LDS_BYTES = 81920;
constexpr int A1_PRIO = 2, A2_PRIO = 0, G1_PRIO = 1, G2_PRIO = 0, G3_PRIO = 2;

__device__ __forceinline__ unsigned cvtpk(float lo, float hi) { f32x2 v = {lo, hi}; bf16x2_t b = __builtin_convertvector(v, bf16x2_t); return __builtin_bit_cast(unsigned, b); }
__device__ __forceinline__ float bf_lo(unsigned u) { return __uint_as_float(u << 16); }
__device__ __forceinline__ float bf_hi(unsigned u) { return __uint_as_float(u & 0xffff0000u); }
constexpr unsigned DMA_BIAS = 4096;
__device__ __forceinline__ unsigned dma_off(unsigned off, int i) { return off + DMA_BIAS - 1024u * (unsigned)(i & 3); }
#define DMA_I(op, o, sfx) "global_load_lds_dwordx4 %" #op ", %0 offset:" #o sfx "\n\t"
#define DMA_DEF(NAME, SFX) \
__device__ __forceinline__ void NAME(const void* sbase, unsigned lds_dst, unsigned v0) { \
    asm volatile("s_mov_b32 m0, %1\n\ts_nop 0\n\t" DMA_I(2, 0, SFX) :: "s"((const char*)sbase - DMA_BIAS), "s"(lds_dst), "v"(v0) : "memory", "m0"); } \
__device__ __forceinline__ void NAME(const void* sbase, unsigned lds_dst, unsigned v0, unsigned v1) { \
    asm volatile("s_mov_b32 m0, %1\n\ts_nop 0\n\t" DMA_I(2, 0, SFX) DMA_I(3, 1024, SFX) :: "s"((const char*)sbase - DMA_BIAS), "s"(lds_dst), "v"(v0), "v"(v1) : "memory", "m0"); } \
__device__ __forceinline__ void NAME(const void* sbase, unsigned lds_dst, unsigned v0, unsigned v1, unsigned v2) { \
    asm volatile("s_mov_b32 m0, %1\n\ts_nop 0\n\t" DMA_I(2, 0, SFX) DMA_I(3, 1024, SFX) DMA_I(4, 2048, SFX) :: "s"((const char*)sbase - DMA_BIAS), "s"(lds_dst), "v"(v0), "v"(v1), "v"(v2) : "memory", "m0"); } \
__device__ __forceinline__ void NAME(const void* sbase, unsigned lds_dst, unsigned v0, unsigned v1, unsigned v2, unsigned v3) { \
    asm volatile("s_mov_b32 m0, %1\n\ts_nop 0\n\t" DMA_I(2, 0, SFX) DMA_I(3, 1024, SFX) DMA_I(4, 2048, SFX) DMA_I(5, 3072, SFX) :: "s"((const char*)sbase - DMA_BIAS), "s"(lds_dst), "v"(v0), "v"(v1), "v"(v2), "v"(v3) : "memory", "m0"); }
DMA_DEF(dma, "")
DMA_DEF(dma_dev, " sc1")
template <int N> __device__ __forceinline__ void wait_vm() { asm volatile("s_waitcnt vmcnt(%0)" :: "i"(N) : "memory"); }
template <int N> __device__ __forceinline__ void wait_vm_bar() { asm volatile("s_waitcnt vmcnt(%0) lgkmcnt(0)\n\ts_barrier" :: "i"(N) : "memory"); }
__device__ __forceinline__ void bar() { asm volatile("s_barrier" ::: "memory"); }
__device__ __forceinline__ void lgkm_bar() { asm volatile("s_waitcnt lgkmcnt(0)\n\ts_barrier" ::: "memory"); }
__device__ __forceinline__ bf16x8 lds_b128(lds_cptr p) { return *(LAS const bf16x8*)p; }
typedef short v4i16_t __attribute__((ext_vector_type(4)));
__device__ __forceinline__ s16x4 lds_tr(lds_cptr p) { return __builtin_bit_cast(s16x4, __builtin_amdgcn_ds_read_tr16_b64_v4i16((LAS v4i16_t*)p)); }
__device__ __forceinline__ bf16x8 cat8(s16x4 a, s16x4 b) { return (bf16x8){a[0], a[1], a[2], a[3], b[0], b[1], b[2], b[3]}; }
#define SBAR() __builtin_amdgcn_sched_barrier(0)
#define PIN(x) asm volatile("" : "+v"(x))
#define MFMA16(a, b, c) __builtin_amdgcn_mfma_f32_16x16x32_bf16(a, b, c, 0, 0, 0)
#define MFMA32(a, b, c) __builtin_amdgcn_mfma_f32_32x32x16_bf16(a, b, c, 0, 0, 0)
#define BC8(x) __builtin_bit_cast(bf16x8, x)
__device__ __forceinline__ float row16_max(float v) {
    v = fmaxf(v, __uint_as_float(__builtin_amdgcn_mov_dpp(__float_as_uint(v), 0x128, 0xf, 0xf, false)));
    v = fmaxf(v, __uint_as_float(__builtin_amdgcn_mov_dpp(__float_as_uint(v), 0x124, 0xf, 0xf, false)));
    v = fmaxf(v, __uint_as_float(__builtin_amdgcn_mov_dpp(__float_as_uint(v), 0x122, 0xf, 0xf, false)));
    v = fmaxf(v, __uint_as_float(__builtin_amdgcn_mov_dpp(__float_as_uint(v), 0x121, 0xf, 0xf, false)));
    return v;
}
__device__ __forceinline__ void st16_dev(rsrc_t r, unsigned off, u32x4 v) { __builtin_amdgcn_raw_buffer_store_b128(v, r, off, 0, 16); }
__device__ __forceinline__ void st8_dev(rsrc_t r, unsigned off, u32x2 v) { __builtin_amdgcn_raw_buffer_store_b64(v, r, off, 0, 16); }
__device__ __forceinline__ void st4_dev(rsrc_t r, unsigned off, unsigned v) { __builtin_amdgcn_raw_buffer_store_b32(v, r, off, 0, 16); }
__device__ __forceinline__ u32x4 ld16_dev(rsrc_t r, unsigned off) { return __builtin_amdgcn_raw_buffer_load_b128(r, off, 0, 16); }
__device__ __forceinline__ unsigned ld4_dev(rsrc_t r, unsigned off) { return __builtin_amdgcn_raw_buffer_load_b32(r, off, 0, 16); }
__device__ __forceinline__ u64 flag_ld(const char* ws, unsigned off) { return __hip_atomic_load((GAS const u64*)(ws + off), __ATOMIC_RELAXED, __HIP_MEMORY_SCOPE_AGENT); }
__device__ __forceinline__ void flag_st(char* ws, unsigned off, u64 v) { __hip_atomic_store((GAS u64*)(ws + off), v, __ATOMIC_RELAXED, __HIP_MEMORY_SCOPE_AGENT); }
__device__ __forceinline__ void poll_flags2(const char* ws, unsigned off0, unsigned off1, u64 epoch) {
    const u64 tag = epoch + 1;
    for (unsigned spins = 0; spins < (1u << 16); ++spins) {
        const u64 a = flag_ld(ws, off0), b = flag_ld(ws, off1);
        if (__all(a == tag && b == tag)) break;
        __builtin_amdgcn_s_sleep(8);
    }
}
__device__ __forceinline__ void poll_flags(const char* ws, unsigned off, u64 epoch) {
    const u64 tag = epoch + 1;
    for (unsigned spins = 0; spins < (1u << 16); ++spins) {
        if (__all(flag_ld(ws, off) == tag)) break;
        __builtin_amdgcn_s_sleep(8);
    }
}
__device__ __forceinline__ int lane_id() { int l; asm volatile("v_mbcnt_lo_u32_b32 %0, -1, 0\n\tv_mbcnt_hi_u32_b32 %0, -1, %0" : "=v"(l)); return l; }
__device__ __forceinline__ u64 publish(char* ws, unsigned off, u64 epoch) {
    asm volatile("s_waitcnt vmcnt(0) lgkmcnt(0)\n\ts_barrier" : "+v"(epoch) :: "memory");
    const u64 e = (u64)(unsigned)__builtin_amdgcn_readfirstlane((unsigned)epoch) | ((u64)(unsigned)__builtin_amdgcn_readfirstlane((unsigned)(epoch >> 32)) << 32);
    if (threadIdx.x == 0) flag_st(ws, off, e + 1);
    return e;
}

constexpr int A1_XSL = 16384, A1_WOFF = 3 * A1_XSL, A1_WSL = 8192;

__device__ __forceinline__ void a1_loader(const u16* __restrict__ x, const u16* __restrict__ wqkv, char* smem, int lane, int l, int rb, int j) {
    const unsigned lds0 = (unsigned)(uintptr_t)smem;
    const int l15 = lane & 15, g4 = lane >> 4;
    unsigned xo[4], qo[2];
#pragma unroll
    for (int i = 0; i < 4; ++i) { const int row = 32 * l + 8 * i + (lane >> 3), ch = (lane & 7) ^ ((row >> 1) & 7); xo[i] = dma_off((unsigned)((rb * 128 + row) * D + ch * 8) * 2u, i & 1); }
#pragma unroll
    for (int i = 0; i < 2; ++i) { const int row = 8 * l + 4 * i + g4, f = (row & 3) | (((row >> 3) & 1) << 2), ch = l15 ^ (f << 1); qo[i] = dma_off((unsigned)(row * NQKV + j * 128 + ch * 8) * 2u, i); }
    auto W = [&](int s) { const char* qb = (const char*)wqkv + (size_t)s * (32 * NQKV * 2); const unsigned d = lds0 + A1_WOFF + (s & 3) * A1_WSL + l * 2048;
                          dma(qb, d, qo[0], qo[1]); };
    int xslot = 0;
    auto Xa = [&](int i) { dma((const char*)x + i * 128, lds0 + xslot + l * 4096, xo[0], xo[1]); };
    auto Xb = [&](int i) { dma((const char*)x + i * 128, lds0 + xslot + l * 4096 + 2048, xo[2], xo[3]); xslot += A1_XSL; if (xslot == 3 * A1_XSL) xslot = 0; };
    Xa(0); Xb(0); W(0); W(1);
    wait_vm_bar<2>();
    Xa(1); Xb(1); W(2); W(3); Xa(2); Xb(2);
    wait_vm_bar<12>();
    wait_vm_bar<6>(); W(4);
    wait_vm_bar<6>(); W(5); Xa(3);
    wait_vm_bar<4>(); W(6); Xb(3);
#pragma unroll 1
    for (int s = 4; s < 16; s += 2) {
        wait_vm_bar<6>(); W(s + 3); Xa(s / 2 + 2);
        wait_vm_bar<4>(); W(s + 4); Xb(s / 2 + 2);
    }
    wait_vm_bar<6>(); W(19);
    wait_vm_bar<2>();
    wait_vm_bar<0>();
    lgkm_bar();
}

template <bool VT>
__device__ __forceinline__ void a1_qkv(const u16* __restrict__ x, const u16* __restrict__ wqkv, char* __restrict__ ws, rsrc_t wsr, char* smem, int lane, int wid, int rb, int j) {
    const lds_cptr L = (lds_cptr)smem;
    const int l15 = lane & 15, g4 = lane >> 4, wm = wid & 1, wn = (wid >> 1) & 1;
    f32x4 acc[4][4];
#pragma unroll
    for (int a = 0; a < 4; ++a)
#pragma unroll
        for (int b = 0; b < 4; ++b) acc[a][b] = (f32x4){0.f, 0.f, 0.f, 0.f};
    if (wid >= 4) a1_loader(x, wqkv, smem, lane, wid - 4, rb, j);
    else {
        const int xrd = (wm * 64 + l15) * 128 + ((g4 ^ ((l15 >> 1) & 7)) << 4);
        int wrd[4];
#pragma unroll
        for (int n = 0; n < 4; ++n) { const int q = l15 >> 2, p = l15 & 3, ch = (2 * (wn * 4 + n) + (p >> 1)) ^ (2 * q | ((g4 & 1) << 3)); wrd[n] = A1_WOFF + (8 * g4 + q) * 256 + ch * 16 + (p & 1) * 8; }
        bf16x8 xf[2][4], wf[4];
        int xo_ = 0, wo_ = 0;
        lds_cptr xb_, wb_;
        auto rdcur = [&](int half) { xb_ = L + xo_ + (xrd ^ (half << 6)); wb_ = L + wo_; };
        auto adv = [&](int half) { wo_ = (wo_ + A1_WSL) & (4 * A1_WSL - 1); if (half) { xo_ += A1_XSL; if (xo_ == 3 * A1_XSL) xo_ = 0; } };
        auto rdx = [&](int b, int t) { xf[b][t] = lds_b128(xb_ + t * 2048); };
        auto rdw = [&](int n) { wf[n] = cat8(lds_tr(wb_ + wrd[n]), lds_tr(wb_ + wrd[n] + 1024)); };
        auto mmn = [&](int b, int n) {
#pragma unroll
            for (int t = 0; t < 4; ++t) acc[n][t] = VT ? MFMA16(xf[b][t], wf[n], acc[n][t]) : MFMA16(wf[n], xf[b][t], acc[n][t]);
        };
        lgkm_bar();
        rdcur(0);
#pragma unroll
        for (int t = 0; t < 4; ++t) { rdx(0, t); rdw(t); }
        adv(0); rdcur(1);
        __builtin_amdgcn_s_waitcnt(0xC07F);
#define A1_STEP(B) do { bar(); SBAR(); \
            mmn(B, 0); SBAR(); rdx(B ^ 1, 0); rdx(B ^ 1, 1); rdw(0); SBAR(); \
            mmn(B, 1); SBAR(); rdx(B ^ 1, 2); rdx(B ^ 1, 3); rdw(1); SBAR(); \
            mmn(B, 2); SBAR(); rdw(2); SBAR(); \
            mmn(B, 3); SBAR(); rdw(3); adv(B ^ 1); rdcur(B); SBAR(); } while (0)
#pragma unroll 1
        for (int i = 0; i < 10; ++i) { A1_STEP(0); A1_STEP(1); }
#undef A1_STEP
    }
    if constexpr (!VT) {
        if (wid < 4) {
            const bool isK = j >= 5; const float sc = isK ? 1.f : QSCALE; const int cb = (isK ? j - 5 : j) * 128;
            const unsigned img = isK ? WS_KV : WS_Q, istr = isK ? 3072u : 1536u, c1 = isK ? 0x3f803f80u : 0u;
            float kmx = 0.f;
#pragma unroll
            for (int n = 0; n < 4; ++n) {
                const int oc = (cb + wn * 64 + n * 16) / 8 + (g4 >> 1), m = oc / 5, r = oc - m * 5;
                const bool mixed = r == 2;
                const int head = 2 * m + (r > 2 || (mixed && (g4 & 1)) ? 1 : 0);
                const unsigned sub = mixed ? 1024u : ((r == 1 || r == 4) ? 512u : 0u);
                float mx = 0.f;
#pragma unroll
                for (int tp = 0; tp < 2; ++tp) {
                    unsigned w[2][2];
#pragma unroll
                    for (int z = 0; z < 2; ++z) {
                        const f32x4 v = acc[n][2 * tp + z];
                        mx = fmaxf(mx, v[0] * v[0] + v[1] * v[1] + v[2] * v[2] + v[3] * v[3]);
                        w[z][0] = cvtpk(v[0] * sc, v[1] * sc); w[z][1] = cvtpk(v[2] * sc, v[3] * sc);
                    }
                    const auto r0 = __builtin_amdgcn_permlane16_swap(w[0][0], w[1][0], false, false), r1 = __builtin_amdgcn_permlane16_swap(w[0][1], w[1][1], false, false);
                    const unsigned tile = img + (unsigned)(head * 64 + rb * 4 + wm * 2 + tp) * istr + sub;
                    if (!mixed) st16_dev(wsr, tile + ((g4 & 1) * 16 + l15) * 16, (u32x4){r0[0], r1[0], r0[1], r1[1]});
                    else { st16_dev(wsr, tile + l15 * 16, (u32x4){w[0][0], w[0][1], c1, 0u}); st16_dev(wsr, tile + (16 + l15) * 16, (u32x4){w[1][0], w[1][1], c1, 0u}); }
                }
                if (isK) { mx = row16_max(mx); if (l15 == n) kmx = mx; }
            }
            if (isK && l15 < 4) st4_dev(wsr, WS_KMAX + (unsigned)((((j - 5) * 32 + rb * 2 + wm) * 32) + wn * 16 + l15 * 4 + g4) * 4, __float_as_uint(kmx));
        }
    } else if (wid < 4) {
#pragma unroll
        for (int n = 0; n < 4; ++n) {
            const int c = (j - 10) * 128 + wn * 64 + n * 16 + l15; const int head = c / 20, row = c - head * 20;
#pragma unroll
            for (int T = 0; T < 2; ++T) {
                const f32x4 va = acc[n][2 * T], vb = acc[n][2 * T + 1];
                const auto r0 = __builtin_amdgcn_permlane32_swap(cvtpk(va[0], va[1]), cvtpk(vb[0], vb[1]), false, false);
                const auto r1 = __builtin_amdgcn_permlane32_swap(cvtpk(va[2], va[3]), cvtpk(vb[2], vb[3]), false, false);
                const unsigned dst = WS_KV + (unsigned)(head * 64 + rb * 4 + wm * 2 + T) * 3072u + 1536 + (((lane >> 5) * 2 + (g4 & 1)) * 24) * 16;
                st16_dev(wsr, dst + row * 16, (u32x4){r0[0], r1[0], r0[1], r1[1]});
                if (row < 4) { const unsigned cc = row == 0 ? 0x3f803f80u : 0u; st16_dev(wsr, dst + (20 + row) * 16, (u32x4){cc, cc, cc, cc}); }
            }
        }
    }
}

constexpr int A2_CH = 8 * 3072, A2_NSL = 3, A2_MISC = A2_NSL * A2_CH;
constexpr float L_MIN = 5.421010862427522e-20f;

__device__ __forceinline__ void attn_issue(const char* kvh, unsigned lds0, int wid, unsigned voff, int c) {
    const unsigned v = voff + DMA_BIAS;
    dma_dev(kvh + (size_t)c * A2_CH + wid * 3072, lds0 + (c % A2_NSL) * A2_CH + wid * 3072, v, v, v);
}
__device__ __forceinline__ float attn_maxpass(const char* kvh, unsigned lds0, lds_cptr L, int wid, int lane, bf16x8 q0, bf16x8 q1) {
    const int l31 = lane & 31, hi = lane >> 5; const unsigned voff = (unsigned)(lane * 16);
    const int k0o = hi * 512 + l31 * 16, k1o = 1024 + l31 * 16; float mx = -INFINITY;
    for (int c = 0; c < 8; ++c) {
        attn_issue(kvh, lds0, wid, voff, c);
        wait_vm_bar<0>();
        const lds_cptr base = L + (c % A2_NSL) * A2_CH;
        for (int t = 0; t < 8; ++t) {
            const bf16x8 kf0 = lds_b128(base + t * 3072 + k0o), kf1 = lds_b128(base + t * 3072 + k1o);
            f32x16 s = MFMA32(kf0, q0, (f32x16){}); s = MFMA32(kf1, q1, s);
#pragma unroll
            for (int r = 0; r < 16; ++r) mx = fmaxf(mx, s[r]);
        }
        lgkm_bar();
    }
    return mx;
}
template <int YOUNG>
__device__ __forceinline__ void attn_stream(const char* kvh, unsigned lds0, lds_cptr L, int wid, int lane, bf16x8 q0, bf16x8 q1, f32x16& o) {
    const int l31 = lane & 31, hi = lane >> 5;
    const unsigned voff = (unsigned)(lane * 16);
    const int k0o = hi * 512 + l31 * 16, k1o = hi ? 1536 + 21 * 16 : 1024 + l31 * 16, vo = 1536 + hi * 384 + (l31 > 21 ? 21 : l31) * 16;
    f32x16 sA, sB; u32x4 p0, p1 = {0u, 0u, 0u, 0u}; bf16x8 kf0, kf1, vf1 = {0, 0, 0, 0, 0, 0, 0, 0};
    { const bf16x8 a = lds_b128(L + k0o), b = lds_b128(L + k1o); sA = MFMA32(a, q0, (f32x16){}); sA = MFMA32(b, q1, sA); }
    kf0 = lds_b128(L + 3072 + k0o); kf1 = lds_b128(L + 3072 + k1o);
#define EXP2(SC, i) cvtpk(__builtin_amdgcn_exp2f(SC[i]), __builtin_amdgcn_exp2f(SC[(i) + 1]))
#define PRIO(PH_) __builtin_amdgcn_s_setprio(YOUNG == (PH_) ? A2_PRIO + 1 : A2_PRIO)
#define TILE(SC, SN, VB, KN) do { \
        const bf16x8 nv0 = lds_b128((VB) + vo); SBAR(); \
        o = MFMA32(vf1, BC8(p1), o); p0[0] = EXP2(SC, 0); p0[1] = EXP2(SC, 2); PIN(p0); SBAR(); \
        vf1 = lds_b128((VB) + vo + 768); SBAR(); \
        SN = MFMA32(kf0, q0, (f32x16){}); p0[2] = EXP2(SC, 4); p0[3] = EXP2(SC, 6); PIN(p0); SBAR(); \
        kf0 = lds_b128((KN) + k0o); SBAR(); \
        SN = MFMA32(kf1, q1, SN); p1[0] = EXP2(SC, 8); p1[1] = EXP2(SC, 10); PIN(p1); SBAR(); \
        kf1 = lds_b128((KN) + k1o); SBAR(); \
        o = MFMA32(nv0, BC8(p0), o); p1[2] = EXP2(SC, 12); p1[3] = EXP2(SC, 14); PIN(p1); SBAR(); \
    } while (0)
    __builtin_amdgcn_s_waitcnt(0xC07F);
    int s0_ = 0;
#pragma unroll 1
    for (int c = 0; c < 8; ++c) {
        const int s1_ = s0_ == 2 * A2_CH ? 0 : s0_ + A2_CH;
        const lds_cptr b0 = L + s0_, b1 = L + s1_;
        s0_ = s1_;
        PRIO(1); TILE(sA, sB, b0 + 0 * 3072, b0 + 2 * 3072);
        PRIO(0); TILE(sB, sA, b0 + 1 * 3072, b0 + 3 * 3072);
        PRIO(1); TILE(sA, sB, b0 + 2 * 3072, b0 + 4 * 3072);
        PRIO(0); TILE(sB, sA, b0 + 3 * 3072, b0 + 5 * 3072);
        PRIO(1); TILE(sA, sB, b0 + 4 * 3072, b0 + 6 * 3072);
        PRIO(0); TILE(sB, sA, b0 + 5 * 3072, b0 + 7 * 3072);
        wait_vm_bar<0>();
        const bool more = c + 2 < 8;
        if (more && !YOUNG) attn_issue(kvh, lds0, wid, voff, c + 2);
        PRIO(1); TILE(sA, sB, b0 + 6 * 3072, b1);
        if (more && YOUNG) attn_issue(kvh, lds0, wid, voff, c + 2);
        PRIO(0); TILE(sB, sA, b0 + 7 * 3072, b1 + 3072);
    }
#undef PRIO
    o = MFMA32(vf1, BC8(p1), o);
    lgkm_bar();
#undef TILE
#undef EXP2
}
__device__ __forceinline__ unsigned neg_split(float m) {
    const float mh = __uint_as_float(__float_as_uint(m) & 0xffff0000u); const float ml = m - mh;
    return (__float_as_uint(-mh) >> 16) | (cvtpk(-ml, 0.f) << 16);
}

__device__ __forceinline__ void a2_attn(char* __restrict__ ws, rsrc_t wsr, char* smem, int lane, int wid, int head, int qb, u64 epoch) {
    const unsigned lds0 = (unsigned)(uintptr_t)smem; const lds_cptr L = (lds_cptr)smem;
    const int l31 = lane & 31, hi = lane >> 5;
    const char* kvh = ws + WS_KV + (size_t)head * 64 * 3072;
    if (wid == 0) {
        const int c0 = (head * 20) >> 7, c1 = (head * 20 + 19) >> 7, grp = lane >> 4;
        poll_flags2(ws, WS_FLQ + (unsigned)(((grp < 2 ? 5 : 10) + ((grp & 1) ? c1 : c0)) * 16 + (lane & 15)) * 8,
                    WS_FLQ + (unsigned)(((lane & 4) ? 15 : (lane & 1) ? c1 : c0) * 16 + qb * 2 + ((lane >> 1) & 1)) * 8, epoch);
    }
    asm volatile("s_barrier" ::: "memory");
    const unsigned voff = (unsigned)(lane * 16);
    const int tok = qb * 256 + wid * 32 + l31;
    const unsigned qimg = WS_Q + (unsigned)(head * 64 + qb * 8 + wid) * 1536u;
    u32x4 qa = ld16_dev(wsr, qimg + hi * 512 + l31 * 16), qe = ld16_dev(wsr, qimg + 1024 + l31 * 16);
    unsigned gq = ld4_dev(wsr, WS_G + tok * 4);
    LAS float* misc = (LAS float*)((lds_ptr)smem + A2_MISC);
    if (wid == 7) {
        float kmx = 0.f;
        float kv[3];
#pragma unroll
        for (int z = 0; z < 3; ++z) {
            const int u = head * 5 + (z < 2 ? 2 * z + hi : 4);
            kv[z] = __uint_as_float(ld4_dev(wsr, WS_KMAX + (unsigned)(((u >> 5) * 32 + l31) * 32 + (u & 31)) * 4));
        }
#pragma unroll
        for (int z = 0; z < 3; ++z) {
            float v = row16_max(kv[z]); v = fmaxf(v, __shfl_xor(v, 16));
            if (z < 2) v += __shfl_xor(v, 32);
            kmx += v;
        }
        if (lane == 0) misc[0] = kmx;
    }
    attn_issue(kvh, lds0, wid, voff, 0);
    wait_vm_bar<0>();
    PIN(qa); PIN(qe); PIN(gq);
    attn_issue(kvh, lds0, wid, voff, 1);
    float qn = bf_lo(qa[0]) * bf_lo(qa[0]) + bf_hi(qa[0]) * bf_hi(qa[0]) + bf_lo(qa[1]) * bf_lo(qa[1]) + bf_hi(qa[1]) * bf_hi(qa[1])
             + bf_lo(qa[2]) * bf_lo(qa[2]) + bf_hi(qa[2]) * bf_hi(qa[2]) + bf_lo(qa[3]) * bf_lo(qa[3]) + bf_hi(qa[3]) * bf_hi(qa[3]);
    if (hi == 0) qn += bf_lo(qe[0]) * bf_lo(qe[0]) + bf_hi(qe[0]) * bf_hi(qe[0]) + bf_lo(qe[1]) * bf_lo(qe[1]) + bf_hi(qe[1]) * bf_hi(qe[1]);
    qn += __shfl_xor(qn, 32);
    float m = sqrtf(qn * misc[0]) * 1.02f + 1e-30f;
    const bf16x8 q0 = BC8(qa);
    f32x16 o; float l;
    for (int attempt = 0;; ++attempt) {
        const bf16x8 q1 = BC8((hi == 0 ? (u32x4){qe[0], qe[1], neg_split(m), 0u} : (u32x4){0u, 0u, 0u, 0u}));
        o = (f32x16){};
        if (wid < 4) attn_stream<0>(kvh, lds0, L, wid, lane, q0, q1, o); else attn_stream<1>(kvh, lds0, L, wid, lane, q0, q1, o);
        l = __shfl(o[8], 32 + l31);
        const bool bad = !(l >= L_MIN);
        if (attempt == 1) break;
        LAS unsigned* flag = (LAS unsigned*)(misc + 4);
        if (threadIdx.x == 0) *flag = 0u;
        lgkm_bar();
        if (__any(bad) && lane == 0) *flag = 1u;
        lgkm_bar();
        const unsigned f = *(volatile LAS unsigned*)flag;
        lgkm_bar();
        if (!f) break;
        const bf16x8 q1z = BC8((hi == 0 ? (u32x4){qe[0], qe[1], 0u, 0u} : (u32x4){0u, 0u, 0u, 0u}));
        float mx = attn_maxpass(kvh, lds0, L, wid, lane, q0, q1z);
        m = fmaxf(mx, __shfl_xor(mx, 32));
        attn_issue(kvh, lds0, wid, voff, 0);
        wait_vm_bar<0>();
        attn_issue(kvh, lds0, wid, voff, 1);
    }
    const float sc = __uint_as_float(gq) * __builtin_amdgcn_rcpf(l);
#pragma unroll
    for (int b = 0; b < 3; ++b) {
        if (b < 2 || hi == 0) {
            const int c = head * DH + b * 8 + hi * 4;
            st8_dev(wsr, WS_O + (unsigned)((c >> 4) * S + tok) * 32 + (c & 15) * 2, (u32x2){cvtpk(o[4 * b] * sc, o[4 * b + 1] * sc), cvtpk(o[4 * b + 2] * sc, o[4 * b + 3] * sc)});
        }
    }
    asm volatile("s_waitcnt vmcnt(0)" ::: "memory");
    if (lane == 0) flag_st(ws, WS_FLO + (unsigned)((qb * 8 + wid) * 32 + head) * 8, epoch + 1);
}

constexpr int G1_XSL = 16384, G1_WOFF = 2 * G1_XSL, G1_WSL = 12288, G1_U = 6144;

__device__ __forceinline__ void g1_loader(const u16* __restrict__ x, const u16* __restrict__ wg, const u16* __restrict__ wu, char* smem, int lane, int l, int rb, int j) {
    const unsigned lds0 = (unsigned)(uintptr_t)smem;
    unsigned wo_[6], xo[4];
#pragma unroll
    for (int i = 0; i < 6; ++i) { const int p = i * 64 + lane, row = p / 12, ch = (p - row * 12) ^ (((row >> 3) & 1) << 1); wo_[i] = dma_off((unsigned)(row * FF + j * 96 + ch * 8) * 2u, i); }
#pragma unroll
    for (int i = 0; i < 4; ++i) { const int row = 8 * i + (lane >> 3), ch = (lane & 7) ^ ((row >> 1) & 7); xo[i] = dma_off((unsigned)((rb * 128 + row) * D + ch * 8) * 2u, i); }
    auto half = [&](const u16* w, int s, unsigned off) { const char* b = (const char*)w + (size_t)s * (32 * FF * 2); const unsigned d = lds0 + G1_WOFF + (s & 3) * G1_WSL + off;
        dma(b, d, wo_[0], wo_[1], wo_[2], wo_[3]); dma(b, d + 4096, wo_[4], wo_[5]); };
    auto X = [&](int i) { const char* b = (const char*)x + i * 128; const unsigned d = lds0 + (i & 1) * G1_XSL;
#pragma unroll
        for (int q = 0; q < 4; ++q) dma(b + q * (32 * D * 2), d + q * 4096, xo[0], xo[1], xo[2], xo[3]); };
    if (l == 0) {
        X(0); half(wg, 0, 0);
        wait_vm_bar<0>();
        X(1); half(wg, 2, 0);
#pragma unroll 1
        for (int s = 0; s < 16; s += 2) {
            bar(); half(wg, s + 4, 0);
            wait_vm_bar<6>(); X((s + 4) / 2);
        }
        bar();
        wait_vm_bar<0>();
        bar();
    } else {
        half(wu, 0, G1_U); half(wg, 1, 0); half(wu, 1, G1_U);
        wait_vm_bar<12>();
        half(wu, 2, G1_U); half(wg, 3, 0); half(wu, 3, G1_U);
#pragma unroll 1
        for (int s = 0; s < 16; s += 2) {
            wait_vm_bar<18>(); half(wu, s + 4, G1_U);
            wait_vm_bar<18>(); half(wg, s + 5, 0); half(wu, s + 5, G1_U);
        }
        wait_vm_bar<18>();
        wait_vm_bar<12>();
        wait_vm_bar<0>();
    }
    lgkm_bar();
}

__device__ __forceinline__ void g1_gu(const u16* __restrict__ x, const u16* __restrict__ wg, const u16* __restrict__ wu,
                                      rsrc_t wsr, char* smem, int lane, int wid, int rb, int j) {
    if (wid >= 6) { g1_loader(x, wg, wu, smem, lane, wid - 6, rb, j); return; }
    const lds_cptr L = (lds_cptr)smem;
    const int l15 = lane & 15, g4 = lane >> 4, tm = wid & 1, nh = wid >> 1;
    const int xrd = (tm * 64 + l15) * 128 + ((g4 ^ ((l15 >> 1) & 7)) << 4);
    int wrd[2];
#pragma unroll
    for (int n = 0; n < 2; ++n) wrd[n] = G1_WOFF + (8 * g4 + (l15 >> 2)) * 192 + (((nh * 2 + n) ^ (g4 & 1)) * 32) + (l15 & 3) * 8;
    f32x4 acc[4][4];
#pragma unroll
    for (int a = 0; a < 4; ++a)
#pragma unroll
        for (int b = 0; b < 4; ++b) acc[a][b] = (f32x4){0.f, 0.f, 0.f, 0.f};
    bf16x8 xf[2][4], wf[4];
    lds_cptr xb_, wb_;
    auto rdx = [&](int b, int t) { xf[b][t] = lds_b128(xb_ + (xrd ^ (b << 6)) + t * 2048); };
    auto rdw = [&](int n) { const lds_cptr p = wb_ + wrd[n & 1] + (n >> 1) * G1_U; wf[n] = cat8(lds_tr(p), lds_tr(p + 768)); };
    auto mm = [&](int b, int n) {
#pragma unroll
        for (int t = 0; t < 4; ++t) acc[n][t] = MFMA16(wf[n], xf[b][t], acc[n][t]); };
    unsigned pf0 = 0;
    const char* pfp = (const char*)((lane & 8) ? wu : wg) + (size_t)(((rb & 7) * 4 + ((lane >> 1) & 3)) * FF * 2 + ((j * 192) & ~127) + (lane & 1) * 128);
    auto pf = [&](int s) { if (s < 20 && lane < 16 && wid < 4) asm volatile("global_load_dword %0, %1, off" : "+v"(pf0) : "v"(pfp + (size_t)s * (32 * FF * 2)) : "memory"); };
    pf(4 + wid); if (wid < 2) pf(8 + wid);
    lgkm_bar();
    xb_ = L; wb_ = L;
#pragma unroll
    for (int t = 0; t < 4; ++t) { rdx(0, t); rdw(t); }
    int wo_ = G1_WSL, xo_ = 0;
    __builtin_amdgcn_s_waitcnt(0xC07F);
#define G1_STEP(B) do { lgkm_bar(); xb_ = L + xo_; wb_ = L + wo_; SBAR(); \
        mm(B, 0); SBAR(); rdx(B ^ 1, 0); rdx(B ^ 1, 1); rdw(0); SBAR(); \
        mm(B, 1); SBAR(); rdx(B ^ 1, 2); rdx(B ^ 1, 3); rdw(1); SBAR(); \
        mm(B, 2); SBAR(); rdw(2); wo_ = (wo_ == 3 * G1_WSL) ? 0 : wo_ + G1_WSL; if (B == 0) xo_ ^= G1_XSL; SBAR(); \
        mm(B, 3); SBAR(); rdw(3); SBAR(); } while (0)
#pragma unroll 1
    for (int i = 0; i < 10; ++i) { if (((2 * i) & 3) == (wid & 2)) pf(2 * i + 10 + (wid & 1)); G1_STEP(0); G1_STEP(1); }
    asm volatile("s_waitcnt vmcnt(0)" : "+v"(pf0) :: "memory");
#undef G1_STEP
#pragma unroll
    for (int n = 0; n < 2; ++n) {
#pragma unroll
        for (int tp = 0; tp < 2; ++tp) {
            unsigned w[2][2];
#pragma unroll
            for (int z = 0; z < 2; ++z) {
                float h[4];
#pragma unroll
                for (int e = 0; e < 4; ++e) {
                    const float gt = acc[n][2 * tp + z][e], up = acc[2 + n][2 * tp + z][e];
                    const float u = gt * (1.f + 0.044715f * gt * gt) * (-2.f * 0.7978845608028654f * 1.4426950408889634f);
                    h[e] = gt * __builtin_amdgcn_rcpf(1.f + __builtin_amdgcn_exp2f(u)) * up;
                }
                w[z][0] = cvtpk(h[0], h[1]); w[z][1] = cvtpk(h[2], h[3]);
            }
            const auto r0 = __builtin_amdgcn_permlane16_swap(w[0][0], w[1][0], false, false), r1 = __builtin_amdgcn_permlane16_swap(w[0][1], w[1][1], false, false);
            const int tok = rb * 128 + tm * 64 + (2 * tp + (g4 & 1)) * 16 + l15;
            st16_dev(wsr, WS_H + (unsigned)((j * 6 + nh * 2 + n) * S + tok) * 32 + (g4 >> 1) * 16, (u32x4){r0[0], r1[0], r0[1], r1[1]});
        }
    }
}

constexpr int G2_ST = 9216;
__device__ __forceinline__ void g2_out(const u16* __restrict__ wo, const u16* __restrict__ wd, char* __restrict__ ws, rsrc_t wsr, char* smem, u16* __restrict__ out,
                                       int lane, int wid, int mt, int nt, u64 epoch, bool updater) {
    const int l15 = lane & 15, g4 = lane >> 4, t = wid & 3;
    const bool loader = wid >= 4;
    const unsigned lds0 = (unsigned)(uintptr_t)smem; const lds_cptr L = (lds_cptr)smem;
    unsigned wo_[5];
#pragma unroll
    for (int i = 0; i < 5; ++i) { const int p = 64 * i + lane, row = p / 10, ch = p - row * 10; wo_[i] = dma_off((unsigned)(row * D + nt * 80 + ch * 8) * 2u, i); }
    const unsigned ao = (unsigned)lane * 16u + DMA_BIAS;
    auto issueW = [&](const u16* w, int q, unsigned dst) { const char* wb = (const char*)w + (size_t)q * (32 * D * 2);
        dma(wb, dst, wo_[0], wo_[1], wo_[2], wo_[3]); dma(wb, dst + 4096, wo_[4]); };
    auto issueH = [&](int q, unsigned dst) { const char* ab = ws + WS_H + ((size_t)(2 * q) * S + mt * 64) * 32;
        dma_dev(ab, dst, ao, ao, ao + S * 32 - 2048, ao + S * 32 - 2048); };
    const int wrd = (8 * g4 + (l15 >> 2)) * 160 + (l15 & 3) * 8;
    const int ard = 5120 + (g4 >> 1) * 2048 + (t * 16 + l15) * 32 + (g4 & 1) * 16;
    f32x4 acc[5];
#pragma unroll
    for (int a = 0; a < 5; ++a) acc[a] = (f32x4){0.f, 0.f, 0.f, 0.f};
    if (loader) { issueW(wd, t, lds0 + t * G2_ST); issueW(wd, t + 4, lds0 + (t + 4) * G2_ST); }
    if (wid == 0) poll_flags2(ws, WS_FLH + (unsigned)((mt >> 1) * 16 + l15) * 8, WS_FLQ + (unsigned)(15 * 16 + (mt >> 1)) * 8, epoch);
    if (updater && wid == 1) {
#pragma unroll 1
        for (int i = 0; i < 8; ++i) poll_flags(ws, WS_FLQ + (unsigned)(i * 64 + lane) * 8, epoch);
        if (lane == 0) flag_st(ws, WS_EPOCH, epoch + 1);
    }
    asm volatile("s_barrier" ::: "memory");
    if (loader) {
        issueH(t, lds0 + t * G2_ST + 5120); if (t < 2) issueH(t + 4, lds0 + (t + 4) * G2_ST + 5120);
        if (t < 2) wait_vm<4>();
        bar();
#pragma unroll 1
        for (int r = 0; r < 24; ++r) {
            if (((r + 1) & 1) == (t >> 1)) wait_vm<0>();
            bar();
            const int qn = 2 * (r + 3) + (t & 1);
            if (((r + 3) & 1) == (t >> 1) && qn < 48) { const unsigned d = lds0 + (qn & 7) * G2_ST; if (qn > 7) issueW(wd, qn, d); issueH(qn, d + 5120); }
        }
    } else {
        bf16x8 wf[2][2][5], af[2][2];
        auto rdf = [&](int b, int z, int so) { af[b][z] = lds_b128(L + so + z * G2_ST + ard);
#pragma unroll
            for (int n = 0; n < 5; ++n) wf[b][z][n] = cat8(lds_tr(L + so + z * G2_ST + wrd + n * 32), lds_tr(L + so + z * G2_ST + wrd + 640 + n * 32)); };
        auto mmf = [&](int b, int z) {
#pragma unroll
            for (int n = 0; n < 5; ++n) acc[n] = MFMA16(wf[b][z][n], af[b][z], acc[n]); };
        bar();
        rdf(0, 0, 0); rdf(0, 1, 0);
        int so_ = 2 * G2_ST;
        __builtin_amdgcn_s_waitcnt(0xC07F);
#pragma unroll 1
        for (int r = 0; r < 24; r += 2) {
            bar(); SBAR(); rdf(1, 0, so_); SBAR(); mmf(0, 0); SBAR(); rdf(1, 1, so_); SBAR(); mmf(0, 1); SBAR(); so_ = (so_ + 2 * G2_ST == 8 * G2_ST) ? 0 : so_ + 2 * G2_ST;
            bar(); SBAR(); rdf(0, 0, so_); SBAR(); mmf(1, 0); SBAR(); rdf(0, 1, so_); SBAR(); mmf(1, 1); SBAR(); so_ = (so_ + 2 * G2_ST == 8 * G2_ST) ? 0 : so_ + 2 * G2_ST;
        }
    }
    lgkm_bar();
    const int kh = wid >> 2;
    {
        const int ln = lane_id();
#pragma unroll
        for (int i = 0; i < 5; ++i) { const int p = 64 * i + ln, row = p / 10, ch = p - row * 10; wo_[i] = dma_off((unsigned)(row * D + nt * 80 + ch * 8) * 2u, i); }
    }
    issueW(wo, kh * 10 + 8 + (t & 1), lds0 + (kh * 2 + (t & 1)) * 5120);
    if (!loader) {
        const float omg = 1.f - __uint_as_float(ld4_dev(wsr, WS_G + (unsigned)(mt * 64 + t * 16 + (lane_id() & 15)) * 4));
#pragma unroll
        for (int n = 0; n < 5; ++n) acc[n] *= omg;
    }
    wait_vm_bar<0>();
    bf16x8 wr[2][5];
#pragma unroll
    for (int z = 0; z < 2; ++z)
#pragma unroll
        for (int n = 0; n < 5; ++n) wr[z][n] = cat8(lds_tr(L + (kh * 2 + z) * 5120 + wrd + n * 32), lds_tr(L + (kh * 2 + z) * 5120 + wrd + 640 + n * 32));
    lgkm_bar();
    issueW(wo, kh * 10 + t, lds0 + (kh * 8 + t) * 5120); issueW(wo, kh * 10 + 4 + t, lds0 + (kh * 8 + 4 + t) * 5120);
    __builtin_amdgcn_s_setprio(G3_PRIO);
    if (wid == 0) poll_flags(ws, WS_FLO + (unsigned)(((mt >> 2) * 8 + (mt & 3) * 2 + (lane >> 5)) * 32 + (lane & 31)) * 8, epoch);
    wait_vm_bar<0>();
    {
        const unsigned abase = (unsigned)(((g4 >> 1) + kh * 20) * S + mt * 64 + t * 16 + l15) * 32 + (g4 & 1) * 16;
        u32x4 ao_[10];
#pragma unroll
        for (int i = 0; i < 10; ++i) { const int q = i < 2 ? 8 + i : i - 2; ao_[i] = ld16_dev(wsr, WS_O + (unsigned)q * (2 * S * 32) + abase); }
#pragma unroll
        for (int z = 0; z < 2; ++z)
#pragma unroll
            for (int n = 0; n < 5; ++n) acc[n] = MFMA16(wr[z][n], BC8(ao_[z]), acc[n]);
        bf16x8 wf[2][5];
        auto rdw = [&](int b, int i) {
#pragma unroll
            for (int n = 0; n < 5; ++n) wf[b][n] = cat8(lds_tr(L + (kh * 8 + i) * 5120 + wrd + n * 32), lds_tr(L + (kh * 8 + i) * 5120 + wrd + 640 + n * 32)); };
        rdw(0, 0);
#pragma unroll
        for (int i = 0; i < 8; ++i) {
            if (i + 1 < 8) rdw((i + 1) & 1, i + 1);
            SBAR();
#pragma unroll
            for (int n = 0; n < 5; ++n) acc[n] = MFMA16(wf[i & 1][n], BC8(ao_[2 + i]), acc[n]);
            SBAR();
        }
        lgkm_bar();
        if (kh == 1) {
#pragma unroll
            for (int n = 0; n < 5; ++n) *(LAS f32x4*)((lds_ptr)smem + ((t * 5 + n) * 64 + lane) * 16) = acc[n];
        }
        lgkm_bar();
        if (kh == 0) {
            const int tok = mt * 64 + t * 16 + l15;
#pragma unroll
            for (int n = 0; n < 5; ++n) {
                const f32x4 v = acc[n] + *(LAS const f32x4*)((lds_cptr)smem + ((t * 5 + n) * 64 + lane) * 16);
                *(u32x2*)(out + (size_t)tok * D + nt * 80 + n * 16 + g4 * 4) = (u32x2){cvtpk(v[0], v[1]), cvtpk(v[2], v[3])};
            }
        }
    }
}

__device__ __forceinline__ void a1_gate(const u16* __restrict__ x, const u16* __restrict__ gatew, rsrc_t wsr, int lane, int wid, int rb) {
    const int tok = rb * 128 + wid * 16 + (lane >> 2), part = lane & 3;
    const u32x4* xr = (const u32x4*)(x + (size_t)tok * D) + part; const u32x4* gr = (const u32x4*)gatew + part;
    float acc = 0.f;
#pragma unroll 5
    for (int i = 0; i < 20; ++i) {
        const u32x4 a = xr[4 * i], g = gr[4 * i];
#pragma unroll
        for (int e = 0; e < 4; ++e) acc += bf_lo(a[e]) * bf_lo(g[e]) + bf_hi(a[e]) * bf_hi(g[e]);
    }
    acc += __shfl_xor(acc, 1); acc += __shfl_xor(acc, 2);
    const float gg = __builtin_amdgcn_rcpf(1.f + __builtin_amdgcn_exp2f(-1.4426950408889634f * acc));
    if (part == 0) st4_dev(wsr, WS_G + (unsigned)tok * 4, __float_as_uint(gg));
}

__global__ __launch_bounds__(512, 4) __attribute__((target("no-packed-fp32-ops"))) void mega(const u16* __restrict__ x, const u16* __restrict__ wqkv, const u16* __restrict__ wo, const u16* __restrict__ wg,
                                               const u16* __restrict__ wu, const u16* __restrict__ wd, const u16* __restrict__ gatew, char* __restrict__ ws, u16* __restrict__ out) {
    extern __shared__ __attribute__((aligned(16))) char smem[];
    const int tid = threadIdx.x, lane = tid & 63, wid = __builtin_amdgcn_readfirstlane(tid >> 6);
    const int bid = blockIdx.x, b = bid & 255, xcd = b & 7, bi = b >> 3;
    const int rb = (xcd & 1) * 8 + (bi & 7), j = (xcd >> 1) * 4 + (bi >> 3);
    const rsrc_t wsr = __builtin_amdgcn_make_buffer_rsrc(ws, 0, WS_END, 0x00020000);
    if (bid < 256) {
        const u64 epoch0 = flag_ld(ws, WS_EPOCH);
        __builtin_amdgcn_s_setprio(A1_PRIO);
        if (j == 15) a1_gate(x, gatew, wsr, lane, wid, rb);
        else if (j >= 10) a1_qkv<true>(x, wqkv, ws, wsr, smem, lane, wid, rb, j);
        else a1_qkv<false>(x, wqkv, ws, wsr, smem, lane, wid, rb, j);
        const u64 epoch = publish(ws, WS_FLQ + (unsigned)(j * 16 + rb) * 8, epoch0);
        __builtin_amdgcn_s_setprio(A2_PRIO);
        a2_attn(ws, wsr, smem, lane, wid, (b & 7) * 4 + ((b >> 3) & 3), b >> 5, epoch);
    } else {
        const u64 epoch0 = flag_ld(ws, WS_EPOCH);
        __builtin_amdgcn_s_setprio(G1_PRIO);
        g1_gu(x, wg, wu, wsr, smem, lane, wid, rb, j);
        const u64 epoch = publish(ws, WS_FLH + (unsigned)(rb * 16 + j) * 8, epoch0);
        __builtin_amdgcn_s_setprio(G2_PRIO);
        g2_out(wo, wd, ws, wsr, smem, out, lane, wid, (xcd >> 1) * 8 + (bi & 7), (xcd & 1) * 4 + (bi >> 3), epoch, b == 255);
    }
}

extern "C" void kernel_launch(void* const* d_in, const int* in_sizes, int n_in, void* d_out, int out_size, void* d_ws, size_t ws_size, hipStream_t stream) {
    const u16* x = (const u16*)d_in[0]; const u16* wqkv = (const u16*)d_in[1]; const u16* wo = (const u16*)d_in[2];
    const u16* wg = (const u16*)d_in[4]; const u16* wu = (const u16*)d_in[5]; const u16* wd = (const u16*)d_in[6]; const u16* gw = (const u16*)d_in[7];
    (void)hipFuncSetAttribute((const void*)mega, hipFuncAttributeMaxDynamicSharedMemorySize, LDS_BYTES);
    mega<<<512, 512, LDS_BYTES, stream>>>(x, wqkv, wo, wg, wu, wd, gw, (char*)d_ws, (u16*)d_out);
}
```
